```python
import torch

def _softmax(x, axis=-1):
    return torch.softmax(x, dim=axis)

def _gelu(x):
    return torch.nn.functional.gelu(x, approximate='tanh')

def _silu(x):
    return torch.nn.functional.silu(x)

def _sigmoid(x):
    return torch.sigmoid(x)

def _layernorm(x, axis=-1, eps=1e-05):
    m = x.mean(dim=axis, keepdim=True)
    v = x.var(dim=axis, keepdim=True, unbiased=False)
    return (x - m) / torch.sqrt(v + eps)

def _scatter_add(tgt, idx, src, axis=0):
    return tgt.clone().index_add_(axis, idx, src)

def _segment_sum(src, seg, n):
    out = torch.zeros((n,) + src.shape[1:], dtype=src.dtype)
    return out.index_add_(0, seg, src)

def _pad(x, pw):
    flat = tuple((v for pair in reversed(pw) for v in pair))
    return torch.nn.functional.pad(x, flat)
kernel_name = 'concat_heads__softmax_full_mha__memory_aug_recurrent_mem_tr__prompt'

def setup_inputs():

    _w = lambda a: (lambda t: t.to(torch.bfloat16) if t.is_floating_point() else t)(torch.as_tensor(a))

    def _A_setup():
        B, S, D = (1, 2048, 576)
        return {'x': _w((lambda *s: torch.randn(*s, dtype=torch.bfloat16) * 0.2)(B, S, D)), 'wqkv': _w((lambda *s: torch.randn(*s, dtype=torch.bfloat16) * 0.2)(D, 3 * D)), 'wo': _w((lambda *s: torch.randn(*s, dtype=torch.bfloat16) * 0.05)(D, D))}
    _A_in = _A_setup()

    def _B_setup():
        B, S, Dm = (1, 2048, 576)
        H, Dh = (32, 128)
        M, seg = (8, 32)
        seg = min(seg, S)
        S = S // seg * seg
        return {'x': _w((lambda *s: torch.randn(*s, dtype=torch.bfloat16) * 0.05)(B, S, Dm)), 'mem0': _w((lambda *s: torch.randn(*s, dtype=torch.bfloat16) * 0.8)(B, M, Dm)), 'Wq': _w((lambda *s: torch.randn(*s, dtype=torch.bfloat16) * 1.6)(Dm, H, Dh) * Dm ** (-0.5)), 'Wk': _w((lambda *s: torch.randn(*s, dtype=torch.bfloat16) * 1.6)(Dm, H, Dh) * Dm ** (-0.5)), 'Wv': _w((lambda *s: torch.randn(*s, dtype=torch.bfloat16) * 0.8)(Dm, H, Dh) * Dm ** (-0.5)), 'Wo': _w((lambda *s: torch.randn(*s, dtype=torch.bfloat16) * 0.8)(H, Dh, Dm) * (H * Dh) ** (-0.5)), 'seg_len': seg}
    _B_in = _B_setup()
    return {'A_x': _A_in['x'], 'A_wqkv': _A_in['wqkv'], 'A_wo': _A_in['wo'], 'B_x': _B_in['x'], 'B_mem0': _B_in['mem0'], 'B_Wq': _B_in['Wq'], 'B_Wk': _B_in['Wk'], 'B_Wv': _B_in['Wv'], 'B_Wo': _B_in['Wo'], 'B_seg_len': _B_in['seg_len']}

def reference(A_x, A_wqkv, A_wo, B_x, B_mem0, B_Wq, B_Wk, B_Wv, B_Wo, B_seg_len):

    def _A_ref(x, wqkv, wo):
        B, S, D = x.shape
        H = 32
        Dh = D // H
        qkv = (lambda x, *d: x.permute(*d))((x @ wqkv).reshape(B, S, 3, H, Dh), 2, 0, 3, 1, 4)
        q, k, v = (qkv[0], qkv[1], qkv[2])
        scale = Dh ** (-0.5)
        scores = torch.einsum('bhqd,bhkd->bhqk', q, k) * scale
        p = _softmax(scores, axis=-1)
        o = torch.einsum('bhqk,bhkd->bhqd', p, v)
        return (lambda x, *d: x.permute(*d))(o, 0, 2, 1, 3).reshape(B, S, D) @ wo

    def _B_ref(x, mem0, Wq, Wk, Wv, Wo, seg_len):
        B, S, Dm = x.shape
        H, Dh = (Wq.shape[1], Wq.shape[2])
        M = mem0.shape[1]
        scale = Dh ** (-0.5)
        n_seg = S // seg_len
        L = M + seg_len
        idx = torch.arange(L)
        mask = (idx[:, None] < M) | (idx[:, None] >= idx[None, :])
        mem = mem0
        outs = []
        for s in range(n_seg):
            xs = x[:, s * seg_len:(s + 1) * seg_len, :].clone()
            h = torch.concatenate([mem, xs], axis=1)
            q = torch.einsum('btd,dhe->bhte', h, Wq)
            k = torch.einsum('btd,dhe->bhte', h, Wk)
            v = torch.einsum('btd,dhe->bhte', h, Wv)
            scores = torch.einsum('bhqe,bhke->bhqk', q, k) * scale
            scores = torch.where(mask[None, None], scores, -1000000000.0)
            p = _softmax(scores, axis=-1)
            o = torch.einsum('bhqk,bhke->bhqe', p, v)
            o = torch.einsum('bhte,hed->btd', o, Wo)
            mem = o[:, :M, :].clone()
            outs.append(o[:, M:, :])
        return torch.concatenate(outs, axis=1)
    a = _A_ref(A_x, A_wqkv, A_wo)
    b = _B_ref(B_x, B_mem0, B_Wq, B_Wk, B_Wv, B_Wo, B_seg_len)
    return torch.concatenate([a, b], axis=-1)
```

```cpp
#include <hip/hip_runtime.h>
#include <stdint.h>

typedef __bf16 bf16x2 __attribute__((ext_vector_type(2)));
typedef __bf16 bf16x8 __attribute__((ext_vector_type(8)));
typedef __bf16 bf16x16 __attribute__((ext_vector_type(16)));
typedef float f32x2 __attribute__((ext_vector_type(2)));
typedef float f32x4 __attribute__((ext_vector_type(4)));
typedef float f32x16 __attribute__((ext_vector_type(16)));
typedef unsigned u32x2 __attribute__((ext_vector_type(2)));
typedef unsigned u32x4 __attribute__((ext_vector_type(4)));
typedef unsigned u32x8 __attribute__((ext_vector_type(8)));
typedef short s16x4 __attribute__((ext_vector_type(4)));
#define LAS __attribute__((address_space(3)))
#define DEV __device__ __forceinline__
typedef LAS char* ldsp;
typedef __amdgpu_buffer_rsrc_t rsrc_t;

constexpr int RING = 8;
constexpr int SLOT_PH = 114688;
constexpr int P_O = 0, MA_O = 20480, MB_O = 29696, SP_O = 40960, SM_O = 45056;
constexpr int FL = RING * SLOT_PH;
constexpr int F_KX = 0, F_U = 32, F_KM = 64, F_PX = 96, F_Y = 128, F_C = 200, F_POL = 201, F_AKV = 208, F_AO = 272;
constexpr int INIT_BYTES = 1 << 20;
constexpr int C_O = 2 << 20;
constexpr int Z_O = C_O + 72 * 294912;
constexpr int KX_O = Z_O + 64 * 72 * 20480;
constexpr int KM_O = KX_O + 64 * 32 * 8192;
constexpr int PX_O = KM_O + 64 * 32 * 4096;
constexpr int AKV_O = PX_O + 64 * 32 * 2560;
constexpr int AO_O = AKV_O + 64 * 73728;
constexpr int DBG_O = AO_O + 2048 * 1152;
constexpr int WS_END = DBG_O + (1 << 20);
constexpr int LDS_BYTES = 160 * 1024;
constexpr int SC1 = 16;

struct Params {
  const char *ax, *awqkv, *awo, *bx, *mem0, *wq, *wk, *wv, *wo;
  char* out; char* ws;
};

DEV unsigned pk(float a, float b) { f32x2 v = {a, b}; return __builtin_bit_cast(unsigned, __builtin_convertvector(v, bf16x2)); }
DEV unsigned umin_(unsigned a, unsigned b) { return a < b ? a : b; }
DEV unsigned umax_(unsigned a, unsigned b) { return a > b ? a : b; }
DEV unsigned pkg(float a, float b) { return umin_(pk(a, b), 0xFFFFFFFEu); }
DEV bf16x8 as8(u32x4 v) { return __builtin_bit_cast(bf16x8, v); }
DEV f32x4 mfma16(u32x4 a, u32x4 b, f32x4 c) { return __builtin_amdgcn_mfma_f32_16x16x32_bf16(as8(a), as8(b), c, 0, 0, 0); }
DEV f32x16 mfma32(u32x4 a, u32x4 b, f32x16 c) { return __builtin_amdgcn_mfma_f32_32x32x16_bf16(as8(a), as8(b), c, 0, 0, 0); }
DEV f32x4 smf(u32x4 a, u32x8 b, f32x4 c, int idx) { return __builtin_amdgcn_smfmac_f32_16x16x64_bf16(as8(a), __builtin_bit_cast(bf16x16, b), c, idx, 0, 0); }
DEV u32x2 trd(ldsp p) { return __builtin_bit_cast(u32x2, __builtin_amdgcn_ds_read_tr16_b64_v4i16((LAS s16x4*)p)); }
DEV u32x4 ld16(rsrc_t r, int vo, int so) { return __builtin_amdgcn_raw_buffer_load_b128(r, vo, so, SC1); }
DEV u32x2 ld8(rsrc_t r, int vo, int so) { return __builtin_amdgcn_raw_buffer_load_b64(r, vo, so, SC1); }
DEV unsigned ld4(rsrc_t r, int vo, int so) { return __builtin_amdgcn_raw_buffer_load_b32(r, vo, so, SC1); }
DEV void st16(u32x4 v, rsrc_t r, int vo, int so) { __builtin_amdgcn_raw_buffer_store_b128(v, r, vo, so, SC1); }
DEV void st8(u32x2 v, rsrc_t r, int vo, int so) { __builtin_amdgcn_raw_buffer_store_b64(v, r, vo, so, SC1); }
DEV void st4(unsigned v, rsrc_t r, int vo, int so) { __builtin_amdgcn_raw_buffer_store_b32(v, r, vo, so, SC1); }
DEV void dma16(const char* g, ldsp l) { __builtin_amdgcn_global_load_lds((const unsigned*)g, (LAS unsigned*)l, 16, 0, 0); }
DEV void vm0() { asm volatile("s_waitcnt vmcnt(0)" ::: "memory"); }
DEV void bar() { asm volatile("s_waitcnt lgkmcnt(0)\n\ts_barrier" ::: "memory"); }
DEV unsigned rt32() { return (unsigned)__builtin_amdgcn_s_memrealtime(); }
DEV int mapo(int o) { return ((o >> 13) << 14) + (o & 8191); }
template <int CTRL> DEV float dpp(float x) { return __builtin_bit_cast(float, __builtin_amdgcn_update_dpp(0, __builtin_bit_cast(int, x), CTRL, 0xf, 0xf, true)); }
DEV float u2f(unsigned x) { return __builtin_bit_cast(float, x); }
DEV unsigned f2u(float x) { return __builtin_bit_cast(unsigned, x); }
DEV float xmax(float x) {
  auto s = __builtin_amdgcn_permlane16_swap(f2u(x), f2u(x), false, false); x = fmaxf(u2f(s[0]), u2f(s[1]));
  auto t = __builtin_amdgcn_permlane32_swap(f2u(x), f2u(x), false, false); return fmaxf(u2f(t[0]), u2f(t[1]));
}
DEV float x32sum(float x) { auto t = __builtin_amdgcn_permlane32_swap(f2u(x), f2u(x), false, false); return u2f(t[0]) + u2f(t[1]); }
DEV float x16sum(float x) { auto t = __builtin_amdgcn_permlane16_swap(f2u(x), f2u(x), false, false); return u2f(t[0]) + u2f(t[1]); }
DEV float xsum(float x) { return x32sum(x16sum(x)); }
DEV float fold32(float a, float b) { auto t = __builtin_amdgcn_permlane32_swap(f2u(a), f2u(b), false, false); return u2f(t[0]) + u2f(t[1]); }

struct Ctx { rsrc_t ws; unsigned t0; int lane, w, g, c16; };
DEV void relane(Ctx& c) {
  int l; asm volatile("v_mbcnt_lo_u32_b32 %0, -1, 0\n\tv_mbcnt_hi_u32_b32 %0, -1, %0" : "=v"(l));
  c.lane = l; c.g = l >> 4; c.c16 = l & 15;
}
DEV bool expired(const Ctx& c) { return rt32() - c.t0 > 3000000u; }
DEV void wait_flags(const Ctx& c, int f0, int n, int v) {
  int vo = FL + (f0 + (c.lane < n ? c.lane : 0)) * 128;
  for (;;) {
    int x = (int)ld4(c.ws, vo, 0);
    if (__all(x >= v)) break;
    if (expired(c)) break;
    __builtin_amdgcn_s_sleep(4);
  }
  asm volatile("" ::: "memory");
}
DEV void set_flag(const Ctx& c, int f, int v) { if (c.lane == 0) st4((unsigned)v, c.ws, FL + f * 128, 0); }

DEV int swz3(int row) { return (row & 1) | ((((row >> 1) ^ (row >> 2)) & 1) << 1) | (((row >> 3) & 1) << 2); }
DEV void stage_w(const Ctx& c, const char* W, int h, int d0, int nrows, ldsp img) {
  for (int p = c.w; p < nrows / 4; p += 4) {
    int row = p * 4 + c.g;
    int ch = c.c16 ^ (swz3(row) << 1);
    dma16(W + (size_t)(d0 + row) * 8192 + h * 256 + ch * 16, img + p * 1024);
  }
}
DEV ldsp w_addr(ldsp img, int row, int e0, int p) { return img + row * 256 + ((((e0 >> 4) ^ swz3(row)) << 5) | (p << 3)); }

DEV void stage_x(const Ctx& c, const char* X, int s, ldsp img) {
#pragma unroll
  for (int i = 0; i < 9; ++i) {
    int f = c.w * 9 + i, ks = f >> 1, tau = f & 1;
    int tok = 8 * (c.c16 >> 2) + 4 * tau + (c.c16 & 3);
    dma16(X + (size_t)(s * 32 + tok) * 1152 + ks * 64 + c.g * 16, img + f * 1024);
  }
}

#define PIN_W(W) do { _Pragma("unroll") for (int t_ = 0; t_ < 8; ++t_) _Pragma("unroll") for (int i_ = 0; i_ < 4; ++i_) asm volatile("" : "+a"(W[t_][i_])); } while (0)

DEV void project_m(const Ctx& c, rsrc_t src, int vo0, int vo8, int so, int step_t, u32x8 (&W)[9][4], f32x4 (&acc)[4], bool check) {
  const int idx = (c.lane & 8) ? 0xEEEE : 0x4444;
  for (;;) {
    u32x4 m[9];
#pragma unroll
    for (int t = 0; t < 9; ++t) m[t] = ld16(src, (t < 4 ? vo0 : vo8) + t * step_t, so);
    __builtin_amdgcn_sched_barrier(0);
#pragma unroll
    for (int i = 0; i < 4; ++i) { acc[i] = f32x4{0.f, 0.f, 0.f, 0.f}; asm volatile("" : "+v"(acc[i])); }
    unsigned mx = 0;
#pragma unroll
    for (int t = 0; t < 9; ++t) {
#pragma unroll
      for (int i = 0; i < 4; ++i) acc[i] = smf(m[t], W[t][i], acc[i], idx);
      mx = umax_(umax_(mx, m[t].x), m[t].y); mx = umax_(umax_(mx, m[t].z), m[t].w);
    }
    if (!check || !__any(mx == 0xFFFFFFFFu)) break;
    if (expired(c)) break;
  }
}

constexpr float SCL_B = 0.08838834764831845f * 1.4426950408889634f;

DEV void role_score(const Params& p, Ctx c, int h, ldsp lds) {
  relane(c);
  u32x8 W[9][4];
#pragma unroll
  for (int r = 0; r < 3; ++r) {
    stage_w(c, p.wq, h, r * 192, 192, lds);
    stage_w(c, p.wk, h, r * 192, 192, lds + 49152);
    vm0(); bar();
    const int q = (c.lane >> 2) & 3, pp = c.lane & 3;
#pragma unroll
    for (int t = 0; t < 3; ++t)
#pragma unroll
      for (int i = 0; i < 4; ++i) {
        u32x8 v;
#pragma unroll
        for (int k = 0; k < 4; ++k) {
          int row = t * 64 + (k >> 1) * 32 + 16 * (c.g >> 1) + 4 * (c.g & 1) + (k & 1) * 2 + (q & 1) + (q >> 1) * 8;
          u32x2 x = trd(w_addr(lds + (i >> 1) * 49152, row, c.w * 32 + (i & 1) * 16, pp));
          v[2 * k] = x.x; v[2 * k + 1] = x.y;
        }
        W[r * 3 + t][i] = v;
      }
    bar();
  }
  if (h == 0 && c.w == 0) {
    unsigned best[2];
#pragma unroll
    for (int pol = 0; pol < 2; ++pol) {
      unsigned b = 0xFFFFFFFFu;
      for (int k = 0; k < 6; ++k) {
        unsigned long long a = __builtin_amdgcn_s_memtime();
        unsigned v = ld4(c.ws, mapo(SM_O + 9216 + k * 128) + pol * 8192 + (k & 1) * SLOT_PH, 0);
        asm volatile("s_waitcnt vmcnt(0)" :: "v"(v) : "memory");
        unsigned d = (unsigned)(__builtin_amdgcn_s_memtime() - a);
        b = umin_(b, d);
      }
      best[pol] = b;
    }
    set_flag(c, F_POL, best[1] < best[0] ? 1 : 0);
  }
  wait_flags(c, F_POL, 1, 0);
  const int pol = __builtin_amdgcn_readfirstlane((int)ld4(c.ws, FL + F_POL * 128, 0)) & 1;
  const int po = pol * 8192;
  const rsrc_t rm0 = __builtin_amdgcn_make_buffer_rsrc((void*)p.mem0, 0, 9216, 0x00020000);
  const int ma0 = mapo(MA_O + c.lane * 16) + po, ma8 = mapo(MA_O + 4096 + c.lane * 16) - 4096 + po;
  const int r8 = c.c16 & 7;
  const int pxo = mapo(P_O + h * 640 + 128 + c.g * 128 + r8 * 16) + po, pmo = mapo(P_O + h * 640 + r8 * 16 + (c.g & 1) * 8) + po;
  const int prz = mapo(P_O + h * 640 + (c.lane < 40 ? c.lane : 0) * 16) + po;
  u32x4 kx[8];
  if (c.w == 0) {
    wait_flags(c, F_KX + h, 1, 0);
#pragma unroll
    for (int i = 0; i < 8; ++i) kx[i] = ld16(c.ws, KX_O + h * 8192 + i * 1024 + c.lane * 16, 0);
  }
  for (int n = 0; n < 64; ++n) {
    PIN_W(W);
    f32x4 acc[4];
    if (n == 0) { int o = r8 * 1152 + c.g * 32 + (c.c16 >> 3) * 16; project_m(c, rm0, o, o, 0, 128, W, acc, false); }
    else project_m(c, c.ws, ma0, ma8, ((n - 1) & (RING - 1)) * SLOT_PH, 1024, W, acc, true);
    ldsp img = lds + (n & 1) * 8192;
#pragma unroll
    for (int i = 0; i < 4; ++i) {
      int e = (i >> 1) * 128 + c.w * 32 + (i & 1) * 16 + c.c16;
      u32x2 v = {pk(acc[i][0], acc[i][1]), pk(acc[i][2], acc[i][3])};
      *(LAS u32x2*)(img + e * 32 + ((c.g ^ ((e >> 2) & 3)) << 3)) = v;
    }
    bar();
    ldsp ra = img + (c.lane >> 2) * 32 + (((c.lane & 3) ^ c.g) << 3);
    if (c.w == 0 && n < 63) {
      u32x4 qf[4], kf[4];
#pragma unroll
      for (int ks = 0; ks < 4; ++ks) { u32x2 a = trd(ra + ks * 1024), b = trd(ra + ks * 1024 + 512); qf[ks] = u32x4{a.x, a.y, b.x, b.y}; }
#pragma unroll
      for (int ks = 0; ks < 4; ++ks) { u32x2 a = trd(ra + 4096 + ks * 1024), b = trd(ra + 4096 + ks * 1024 + 512); kf[ks] = u32x4{a.x, a.y, b.x, b.y}; }
      f32x4 x1 = {0, 0, 0, 0}, x2 = x1, mm = x1;
#pragma unroll
      for (int ks = 0; ks < 4; ++ks) { x1 = mfma16(kx[ks], qf[ks], x1); x2 = mfma16(kx[4 + ks], qf[ks], x2); }
#pragma unroll
      for (int ks = 0; ks < 4; ++ks) mm = mfma16(kf[ks], qf[ks], mm);
      float s[12];
#pragma unroll
      for (int r = 0; r < 4; ++r) { s[r] = x1[r] + dpp<0x128>(x1[r]); s[4 + r] = x2[r] + dpp<0x128>(x2[r]); s[8 + r] = x32sum(mm[r] + dpp<0x128>(mm[r])); }
      float mx = s[0];
#pragma unroll
      for (int r = 1; r < 12; ++r) mx = fmaxf(mx, s[r]);
      mx = xmax(mx) * SCL_B;
#pragma unroll
      for (int r = 0; r < 12; ++r) s[r] = __builtin_amdgcn_exp2f(s[r] * SCL_B - mx);
      float sum = ((s[0] + s[1]) + (s[2] + s[3])) + ((s[4] + s[5]) + (s[6] + s[7])) + 0.5f * ((s[8] + s[9]) + (s[10] + s[11]));
      float inv = __builtin_amdgcn_rcpf(xsum(sum));
      u32x4 px = {pkg(s[0] * inv, s[1] * inv), pkg(s[2] * inv, s[3] * inv), pkg(s[4] * inv, s[5] * inv), pkg(s[6] * inv, s[7] * inv)};
      u32x2 pm = {pkg(s[8] * inv, s[9] * inv), pkg(s[10] * inv, s[11] * inv)};
      const int so = (n & (RING - 1)) * SLOT_PH;
      if (c.c16 < 8) st16(px, c.ws, pxo, so);
      else if (c.g < 2) st8(pm, c.ws, pmo, so);
      if (c.lane < 40) st16(u32x4{~0u, ~0u, ~0u, ~0u}, c.ws, prz, ((n + RING / 2) & (RING - 1)) * SLOT_PH);
      if (n < 62) {
        wait_flags(c, F_KX + h, 1, n + 1);
#pragma unroll
        for (int i = 0; i < 8; ++i) kx[i] = ld16(c.ws, KX_O + h * 8192 + i * 1024 + c.lane * 16, (n + 1) * 262144);
      }
    } else if (c.w == 1) {
#pragma unroll
      for (int ks = 0; ks < 4; ++ks) {
        u32x2 a = trd(ra + 4096 + ks * 1024), b = trd(ra + 4096 + ks * 1024 + 512);
        st16(u32x4{a.x, a.y, b.x, b.y}, c.ws, KM_O + h * 4096 + ks * 1024 + c.lane * 16, n * 131072);
      }
      vm0();
      set_flag(c, F_KM + h, n);
    }
  }
}

DEV void job_c(const Params& p, Ctx c, int job, ldsp lds) {
  relane(c);
  const int h = job >> 2, n0 = (job & 3) * 144;
  for (int pc = c.w; pc < 40; pc += 4) {
    int q = pc * 64 + c.lane, row = q / 20, ch = q - row * 20;
    dma16(p.wo + (size_t)(h * 128 + row) * 1152 + n0 * 2 + (ch < 18 ? ch : 0) * 16, lds + pc * 1024);
  }
  vm0(); bar();
  u32x4 B[9][4];
  {
    const int q = (c.lane >> 2) & 3, pp = c.lane & 3;
#pragma unroll
    for (int i = 0; i < 9; ++i)
#pragma unroll
      for (int ks = 0; ks < 4; ++ks) {
        ldsp a = lds + (ks * 32 + c.g * 8 + q) * 320 + i * 32 + pp * 8;
        u32x2 x = trd(a), y = trd(a + 4 * 320);
        B[i][ks] = u32x4{x.x, x.y, y.x, y.y};
      }
  }
  const int gr = c.c16 >> 2, rg = c.c16 & 3;
  const int drow = 16 * (gr >> 1) + 4 * (gr & 1) + (rg & 1) + (rg >> 1) * 8;
  const int dst = C_O + (n0 / 8 + (c.c16 >> 3)) * 294912 + (h >> 3) * 73728 + ((h & 7) >> 1) * 2048 + (c.g * 16 + (h & 1) * 8 + (c.c16 & 7)) * 16;
  for (int u = c.w; u < 18; u += 4) {
    u32x4 A[2][4];
#pragma unroll
    for (int tau = 0; tau < 2; ++tau)
#pragma unroll
      for (int ks = 0; ks < 4; ++ks) A[tau][ks] = *(const u32x4*)(p.wv + (size_t)(u * 32 + drow + 2 * tau) * 8192 + h * 256 + ks * 64 + c.g * 16);
#pragma unroll
    for (int i = 0; i < 9; ++i) {
      f32x4 a0 = {0, 0, 0, 0}, a1 = a0;
#pragma unroll
      for (int ks = 0; ks < 4; ++ks) { a0 = mfma16(A[0][ks], B[i][ks], a0); a1 = mfma16(A[1][ks], B[i][ks], a1); }
      st16(u32x4{pk(a0[0], a0[1]), pk(a0[2], a0[3]), pk(a1[0], a1[1]), pk(a1[2], a1[3])}, c.ws, dst + i * 2 * 294912, (u >> 1) * 8192 + (u & 1) * 1024);
    }
  }
  vm0(); bar();
  if (c.w == 0 && c.lane == 0) __hip_atomic_fetch_add((int*)(p.ws + FL + F_C * 128), 1, __ATOMIC_RELAXED, __HIP_MEMORY_SCOPE_AGENT);
}

DEV void role_mixer(const Params& p, Ctx c, int j, ldsp lds) {
  relane(c);
  wait_flags(c, F_C, 1, 127);
  u32x8 W[9][4];
#pragma unroll
  for (int t = 0; t < 9; ++t)
#pragma unroll
    for (int i = 0; i < 4; ++i) {
      int o = C_O + j * 294912 + c.w * 73728 + c.lane * 16;
      u32x4 a = ld16(c.ws, o, t * 8192 + i * 2048), b = ld16(c.ws, o, t * 8192 + i * 2048 + 1024);
      W[t][i] = u32x8{a.x, a.y, a.z, a.w, b.x, b.y, b.z, b.w};
    }
  wait_flags(c, F_POL, 1, 0);
  const int pol = __builtin_amdgcn_readfirstlane((int)ld4(c.ws, FL + F_POL * 128, 0)) & 1;
  const int po = pol * 8192;
  const rsrc_t rm0 = __builtin_amdgcn_make_buffer_rsrc((void*)p.mem0, 0, 9216, 0x00020000);
  const int r8 = c.c16 & 7, blk = c.c16 >> 3;
  const int mb0 = mapo(MB_O + c.lane * 16) + po, mb8 = mapo(MB_O + 4096 + c.lane * 16) - 4096 + po;
  int pxo[4];
#pragma unroll
  for (int i = 0; i < 4; ++i) { int o = 2 * c.g + blk; pxo[i] = mapo(P_O + (c.w * 8 + i * 2 + (o >> 2)) * 640 + 128 + (o & 3) * 128 + r8 * 16) + po; }
  const int hm = c.w * 8 + 2 * (c.g >> 1) + blk;
  const int pm0 = mapo(P_O + hm * 640 + r8 * 16 + (c.g & 1) * 8) + po, pm1 = mapo(P_O + (hm + 4) * 640 + r8 * 16 + (c.g & 1) * 8) + po;
  const int mo = mapo(((c.g & 1) ? MB_O : MA_O) + j * 128 + r8 * 16 + blk * 8 + (c.g >> 1) * 4) + po;
  const int zo = Z_O + j * 20480 + c.w * 5120 + c.lane * 16;
  u32x4 zu[4];
  wait_flags(c, F_U + c.w * 8, 8, 0);
#pragma unroll
  for (int i = 0; i < 4; ++i) zu[i] = ld16(c.ws, zo + i * 1024, 0);
  for (int n = 0; n < 64; ++n) {
    PIN_W(W);
    f32x4 acc[4];
    if (n == 0) { int o = r8 * 1152 + c.g * 32 + blk * 16; project_m(c, rm0, o, o, 0, 128, W, acc, false); }
    else {
      const int idx = (c.lane & 8) ? 0xEEEE : 0x4444;
      const int so = ((n - 1) & (RING - 1)) * SLOT_PH;
      for (;;) {
        u32x4 m[9];
#pragma unroll
        for (int t = 0; t < 9; ++t) m[t] = ld16(c.ws, (t < 3 ? mb0 : mb8) + t * 1024, so);
        __builtin_amdgcn_sched_barrier(0);
#pragma unroll
        for (int i = 0; i < 4; ++i) { acc[i] = f32x4{0.f, 0.f, 0.f, 0.f}; asm volatile("" : "+v"(acc[i])); }
        unsigned mx = 0;
#pragma unroll
        for (int t = 0; t < 9; ++t) {
#pragma unroll
          for (int i = 0; i < 4; ++i) acc[i] = smf(m[t], W[t][i], acc[i], idx);
          mx = umax_(umax_(mx, m[t].x), m[t].y); mx = umax_(umax_(mx, m[t].z), m[t].w);
        }
        if (!__any(mx == 0xFFFFFFFFu)) break;
        if (expired(c)) break;
      }
    }
    u32x4 zy;
    {
      float a[4], b[4];
#pragma unroll
      for (int r = 0; r < 4; ++r) { a[r] = fold32(acc[0][r], acc[1][r]); b[r] = fold32(acc[2][r], acc[3][r]); }
      zy = u32x4{pk(a[0], a[1]), pk(a[2], a[3]), pk(b[0], b[1]), pk(b[2], b[3])};
    }
    st16(zy, c.ws, zo + 4096, n * 1474560);
    if (n < 63) {
      const int so = (n & (RING - 1)) * SLOT_PH;
      f32x4 d;
      for (;;) {
        u32x4 px[4]; u32x2 pa, pb;
#pragma unroll
        for (int i = 0; i < 4; ++i) px[i] = ld16(c.ws, pxo[i], so);
        pa = ld8(c.ws, pm0, so); pb = ld8(c.ws, pm1, so);
        __builtin_amdgcn_sched_barrier(0);
        d = f32x4{0, 0, 0, 0};
        unsigned mx = umax_(umax_(pa.x, pa.y), umax_(pb.x, pb.y));
#pragma unroll
        for (int i = 0; i < 4; ++i) {
          d = mfma16(zu[i], px[i], d);
          mx = umax_(umax_(mx, px[i].x), px[i].y); mx = umax_(umax_(mx, px[i].z), px[i].w);
        }
        d = mfma16(zy, u32x4{pa.x, pa.y, pb.x, pb.y}, d);
        if (!__any(mx == 0xFFFFFFFFu)) break;
        if (expired(c)) break;
      }
      ldsp red = lds + (n & 1) * 2048;
      if ((c.g >> 1) == blk) *(LAS f32x4*)(red + ((c.w * 2 + blk) * 16 + (c.g & 1) * 8 + r8) * 16) = d;
      bar();
      if (c.w == 0) {
        f32x4 a = *(LAS f32x4*)(red + (c.g * 32 + c.c16) * 16), b = *(LAS f32x4*)(red + (c.g * 32 + 16 + c.c16) * 16);
        a += b;
        float s0 = x16sum(fold32(a[0], a[2])), s1 = x16sum(fold32(a[1], a[3]));
        st4(pkg(s0, s1), c.ws, mo, so);
        st4(~0u, c.ws, mo, ((n + RING / 2) & (RING - 1)) * SLOT_PH);
      }
      if (n < 62) {
        wait_flags(c, F_U + c.w * 8, 8, n + 1);
#pragma unroll
        for (int i = 0; i < 4; ++i) zu[i] = ld16(c.ws, zo + i * 1024, (n + 1) * 1474560);
      }
      if (c.w == 1) set_flag(c, F_Y + j, n - 1);
    }
  }
  vm0(); bar();
  if (c.w == 1) set_flag(c, F_Y + j, 63);
}

DEV void load_wt(const Ctx& c, const char* Wg, int h, ldsp lds, u32x4 (&F)[18][2]) {
  stage_w(c, Wg, h, 0, 576, lds);
  vm0(); bar();
  const int q = (c.lane >> 2) & 3, pp = c.lane & 3;
#pragma unroll
  for (int ks = 0; ks < 18; ++ks)
#pragma unroll
    for (int i = 0; i < 2; ++i) {
      int row = ks * 32 + c.g * 8 + q;
      u32x2 x = trd(w_addr(lds, row, c.w * 32 + i * 16, pp)), y = trd(w_addr(lds, row + 4, c.w * 32 + i * 16, pp));
      F[ks][i] = u32x4{x.x, x.y, y.x, y.y};
    }
  bar();
}
DEV u32x4 pk8(f32x4 a, f32x4 b) { return u32x4{pk(a[0], a[1]), pk(a[2], a[3]), pk(b[0], b[1]), pk(b[2], b[3])}; }

DEV void role_vu(const Params& p, Ctx c, int h, ldsp lds) {
  relane(c);
  u32x4 WV[18][2], WO[9][4];
  load_wt(c, p.wv, h, lds, WV);
  for (int pc = c.w; pc < 148; pc += 4) {
    int q = pc * 64 + c.lane, row = q / 74, ch = q - row * 74;
    dma16(p.wo + (size_t)(h * 128 + row) * 1152 + (ch < 72 ? ch : 0) * 16, lds + pc * 1024);
  }
  vm0(); bar();
  {
    const int q = (c.lane >> 2) & 3, pp = c.lane & 3;
#pragma unroll
    for (int i = 0; i < 9; ++i)
#pragma unroll
      for (int ks = 0; ks < 4; ++ks) {
        ldsp a = lds + (ks * 32 + c.g * 4 + q) * 1184 + c.w * 288 + i * 32 + pp * 8;
        u32x2 x = trd(a), y = trd(a + 16 * 1184);
        WO[i][ks] = u32x4{x.x, x.y, y.x, y.y};
      }
  }
  bar();
  ldsp vx = lds + 2 * 36864;
  stage_x(c, p.bx, 0, lds);
  const int dst = Z_O + (c.w * 18 + (c.c16 >> 3)) * 20480 + (h >> 3) * 5120 + ((h & 7) >> 1) * 1024 + ((h & 1) * 4 + c.g) * 128 + (c.c16 & 7) * 16;
  for (int s = 0; s < 64; ++s) {
#pragma unroll
    for (int k = 0; k < 18; ++k) { asm volatile("" : "+a"(WV[k][0])); asm volatile("" : "+a"(WV[k][1])); }
#pragma unroll
    for (int i = 0; i < 7; ++i)
#pragma unroll
      for (int k = 0; k < 4; ++k) asm volatile("" : "+a"(WO[i][k]));
    vm0(); bar();
    if (s > 0 && c.w == 0) set_flag(c, F_U + h, s - 1);
    ldsp xi = lds + (s & 1) * 36864;
    if (s < 63) stage_x(c, p.bx, s + 1, lds + ((s + 1) & 1) * 36864);
    f32x4 v[2][2] = {};
#pragma unroll
    for (int ks = 0; ks < 18; ++ks) {
      u32x4 b0 = *(LAS u32x4*)(xi + ks * 2048 + c.lane * 16), b1 = *(LAS u32x4*)(xi + ks * 2048 + 1024 + c.lane * 16);
#pragma unroll
      for (int i = 0; i < 2; ++i) { v[0][i] = mfma16(WV[ks][i], b0, v[0][i]); v[1][i] = mfma16(WV[ks][i], b1, v[1][i]); }
    }
    *(LAS u32x4*)(vx + c.w * 1024 + c.lane * 16) = pk8(v[0][0], v[0][1]);
    *(LAS u32x4*)(vx + 4096 + c.w * 1024 + c.lane * 16) = pk8(v[1][0], v[1][1]);
    bar();
    u32x4 a[2][4];
#pragma unroll
    for (int ks = 0; ks < 4; ++ks) { a[0][ks] = *(LAS u32x4*)(vx + ks * 1024 + c.lane * 16); a[1][ks] = *(LAS u32x4*)(vx + 4096 + ks * 1024 + c.lane * 16); }
#pragma unroll
    for (int i = 0; i < 9; ++i) {
      f32x4 d0 = {0, 0, 0, 0}, d1 = d0;
#pragma unroll
      for (int ks = 0; ks < 4; ++ks) { d0 = mfma16(a[0][ks], WO[i][ks], d0); d1 = mfma16(a[1][ks], WO[i][ks], d1); }
      st16(pk8(d0, d1), c.ws, dst + i * 40960, s * 1474560);
    }
  }
  vm0(); bar();
  if (c.w == 0) set_flag(c, F_U + h, 63);
}

constexpr int QKW = 4;
DEV void role_qk(const Params& p, Ctx c, int h, ldsp lds) {
  relane(c);
  u32x4 WQ[18][2], WK[18][2];
  load_wt(c, p.wq, h, lds, WQ);
  load_wt(c, p.wk, h, lds, WK);
  ldsp ring = lds + 2 * 36864;
  stage_x(c, p.bx, 0, lds);
  for (int s = 0; s < 64 + QKW; ++s) {
#pragma unroll
    for (int k = 0; k < 18; ++k) { asm volatile("" : "+a"(WQ[k][0])); asm volatile("" : "+a"(WQ[k][1])); }
#pragma unroll
    for (int k = 0; k < 14; ++k) { asm volatile("" : "+a"(WK[k][0])); asm volatile("" : "+a"(WK[k][1])); }
    vm0(); bar();
    if (c.w == 0) {
      if (s > 0 && s <= 64) set_flag(c, F_KX + h, s - 1);
      if (s > QKW) set_flag(c, F_PX + h, s - 1 - QKW);
    }
    if (s < 64) {
      ldsp xi = lds + (s & 1) * 36864;
      if (s < 63) stage_x(c, p.bx, s + 1, lds + ((s + 1) & 1) * 36864);
      f32x4 q[2][2] = {}, k[2][2] = {};
#pragma unroll
      for (int ks = 0; ks < 18; ++ks) {
        u32x4 b0 = *(LAS u32x4*)(xi + ks * 2048 + c.lane * 16), b1 = *(LAS u32x4*)(xi + ks * 2048 + 1024 + c.lane * 16);
#pragma unroll
        for (int i = 0; i < 2; ++i) {
          k[0][i] = mfma16(WK[ks][i], b0, k[0][i]); k[1][i] = mfma16(WK[ks][i], b1, k[1][i]);
          q[0][i] = mfma16(WQ[ks][i], b0, q[0][i]); q[1][i] = mfma16(WQ[ks][i], b1, q[1][i]);
        }
      }
      ldsp r = ring + (s % (QKW + 1)) * 16384 + c.w * 1024 + c.lane * 16;
#pragma unroll
      for (int tau = 0; tau < 2; ++tau) {
        u32x4 kf = pk8(k[tau][0], k[tau][1]);
        st16(kf, c.ws, KX_O + h * 8192 + tau * 4096 + c.w * 1024 + c.lane * 16, s * 262144);
        *(LAS u32x4*)(r + 8192 + tau * 4096) = kf;
        *(LAS u32x4*)(r + tau * 4096) = pk8(q[tau][0], q[tau][1]);
      }
    }
    const int sa = s - QKW;
    if (sa >= 0 && (c.w >> 1) == (sa & 1)) {
      const int tq = c.w & 1;
      wait_flags(c, F_KM + h, 1, sa);
      u32x4 km[4];
#pragma unroll
      for (int ks = 0; ks < 4; ++ks) km[ks] = ld16(c.ws, KM_O + h * 4096 + ks * 1024 + c.lane * 16, sa * 131072);
      ldsp r = ring + (sa % (QKW + 1)) * 16384 + c.lane * 16;
      f32x4 x1 = {0, 0, 0, 0}, x2 = x1, mm = x1;
      u32x4 qf[4];
#pragma unroll
      for (int ks = 0; ks < 4; ++ks) {
        qf[ks] = *(LAS u32x4*)(r + tq * 4096 + ks * 1024);
        x1 = mfma16(*(LAS u32x4*)(r + 8192 + ks * 1024), qf[ks], x1);
        x2 = mfma16(*(LAS u32x4*)(r + 12288 + ks * 1024), qf[ks], x2);
      }
#pragma unroll
      for (int ks = 0; ks < 4; ++ks) mm = mfma16(km[ks], qf[ks], mm);
      const int tokq = 8 * (c.c16 >> 2) + 4 * tq + (c.c16 & 3);
      float sc[12];
#pragma unroll
      for (int r4 = 0; r4 < 4; ++r4) {
        sc[r4] = (8 * c.g + r4 <= tokq) ? x1[r4] * SCL_B : -1.4426950408889634e9f;
        sc[4 + r4] = (8 * c.g + 4 + r4 <= tokq) ? x2[r4] * SCL_B : -1.4426950408889634e9f;
        sc[8 + r4] = x32sum(mm[r4]) * SCL_B;
      }
      float mx = sc[0];
#pragma unroll
      for (int r4 = 1; r4 < 12; ++r4) mx = fmaxf(mx, sc[r4]);
      mx = xmax(mx);
#pragma unroll
      for (int r4 = 0; r4 < 12; ++r4) sc[r4] = __builtin_amdgcn_exp2f(sc[r4] - mx);
      float sum = ((sc[0] + sc[1]) + (sc[2] + sc[3])) + ((sc[4] + sc[5]) + (sc[6] + sc[7])) + 0.5f * ((sc[8] + sc[9]) + (sc[10] + sc[11]));
      float inv = __builtin_amdgcn_rcpf(xsum(sum));
      const int o = PX_O + h * 2560 + tq * 1280;
      st16(u32x4{pk(sc[0] * inv, sc[1] * inv), pk(sc[2] * inv, sc[3] * inv), pk(sc[4] * inv, sc[5] * inv), pk(sc[6] * inv, sc[7] * inv)}, c.ws, o + c.lane * 16, sa * 81920);
      if (c.g < 2) st8(u32x2{pk(sc[8] * inv, sc[9] * inv), pk(sc[10] * inv, sc[11] * inv)}, c.ws, o + 1024 + c.c16 * 16 + c.g * 8, sa * 81920);
    }
  }
  vm0(); bar();
  if (c.w == 0) set_flag(c, F_PX + h, 63);
}

DEV void job_out(const Params& p, Ctx c, int s, int cg, ldsp lds) {
  relane(c);
  wait_flags(c, F_PX + c.w * 8, 8, s);
  wait_flags(c, F_U + c.w * 8, 8, s);
  wait_flags(c, F_Y + cg * 12, 12, s);
  u32x4 B[10][2], A[10][6];
  const int pxs = s * 81920, zs = s * 1474560;
#pragma unroll
  for (int k = 0; k < 8; ++k)
#pragma unroll
    for (int tq = 0; tq < 2; ++tq) B[k][tq] = ld16(c.ws, PX_O + (c.w * 8 + k) * 2560 + tq * 1280 + c.lane * 16, pxs);
#pragma unroll
  for (int k = 0; k < 2; ++k)
#pragma unroll
    for (int tq = 0; tq < 2; ++tq) {
      int o = PX_O + (c.w * 8 + 2 * (c.g >> 1) + k) * 2560 + tq * 1280 + 1024 + c.c16 * 16 + (c.g & 1) * 8;
      u32x2 x = ld8(c.ws, o, pxs), y = ld8(c.ws, o + 4 * 2560, pxs);
      B[8 + k][tq] = u32x4{x.x, x.y, y.x, y.y};
    }
  const int zb = Z_O + (cg * 12 + (c.c16 >> 3)) * 20480 + c.w * 5120 + (c.c16 & 7) * 16;
#pragma unroll
  for (int i = 0; i < 6; ++i) {
#pragma unroll
    for (int k = 0; k < 8; ++k) A[k][i] = ld16(c.ws, zb + i * 40960 + k * 512 + c.g * 128, zs);
#pragma unroll
    for (int k = 0; k < 2; ++k) A[8 + k][i] = ld16(c.ws, zb + i * 40960 + 4096 + c.g * 256 + k * 128, zs);
  }
  __builtin_amdgcn_sched_barrier(0);
#pragma unroll
  for (int i = 0; i < 6; ++i) {
    f32x4 d0 = {0, 0, 0, 0}, d1 = d0;
#pragma unroll
    for (int k = 0; k < 10; ++k) { d0 = mfma16(A[k][i], B[k][0], d0); d1 = mfma16(A[k][i], B[k][1], d1); }
    *(LAS f32x4*)(lds + ((c.w * 12 + i * 2) * 64 + c.lane) * 16) = d0;
    *(LAS f32x4*)(lds + ((c.w * 12 + i * 2 + 1) * 64 + c.lane) * 16) = d1;
  }
  bar();
#pragma unroll
  for (int u = 0; u < 3; ++u) {
    int t = c.w * 3 + u, i = t >> 1, tq = t & 1;
    f32x4 d = *(LAS f32x4*)(lds + (t * 64 + c.lane) * 16);
#pragma unroll
    for (int ww = 1; ww < 4; ++ww) d += *(LAS f32x4*)(lds + ((ww * 12 + t) * 64 + c.lane) * 16);
    int tok = 8 * (c.c16 >> 2) + 4 * tq + (c.c16 & 3);
    *(u32x2*)(p.out + (size_t)(s * 32 + tok) * 2304 + 1152 + (cg * 96 + i * 16 + c.g * 4) * 2) = u32x2{pk(d[0], d[1]), pk(d[2], d[3])};
  }
  bar();
}

DEV void a_wfrags(const Ctx& c, ldsp lds, u32x4 (&W)[18][4], int ti, int tp) {
  const int q = (c.lane >> 2) & 3, pp = c.lane & 3;
#pragma unroll
  for (int ks = 0; ks < 18; ++ks)
#pragma unroll
    for (int i = 0; i < 4; ++i) {
      ldsp a = lds + ((ks >> 1) * 64 + c.g * 16 + (ks & 1) * 8 + q) * 128 + i * ti + pp * tp;
      u32x2 x = trd(a), y = trd(a + 512);
      W[ks][i] = u32x4{x.x, x.y, y.x, y.y};
    }
}
#define PIN_A(W) do { _Pragma("unroll") for (int k_ = 0; k_ < 16; ++k_) _Pragma("unroll") for (int i_ = 0; i_ < 4; ++i_) asm volatile("" : "+a"(W[k_][i_])); } while (0)
template <int AUX> DEV void a_rows(rsrc_t r, int vo, int so, u32x4 (&X)[18]) {
#pragma unroll
  for (int ks = 0; ks < 18; ++ks) X[ks] = __builtin_amdgcn_raw_buffer_load_b128(r, vo + (ks >> 1) * 128 + (ks & 1) * 16, so, AUX);
}
constexpr float SCL_A = 0.23570226039551584f * 1.4426950408889634f;
constexpr int AK1 = 65536, AV = 73728, AONE = 147456;
DEV void a_proj_tile(const Ctx& c, u32x4 (&W)[18][4], u32x4 (&X)[18], int tl, int half, ldsp lds) {
  f32x4 k = {0, 0, 0, 0}, q = k, v = k, e = k;
#pragma unroll
  for (int ks = 0; ks < 18; ++ks) {
    k = mfma16(W[ks][0], X[ks], k); q = mfma16(W[ks][1], X[ks], q); v = mfma16(X[ks], W[ks][2], v); e = mfma16(W[ks][3], X[ks], e);
  }
  const int r = (tl & 1) * 16 + c.c16, fo = ((c.g >> 1) * 32 + r) * 16 + (c.g & 1) * 8;
  ldsp qi = lds + AV + (half ^ 1) * 36864 + (tl >> 1) * 1152, vi = lds + AV + half * 36864 + tl * 576;
  *(LAS u32x2*)(lds + (half * 32 + (tl >> 1)) * 1024 + fo) = u32x2{pk(k[0], k[1]), pk(k[2], k[3])};
  *(LAS u32x2*)(qi + fo) = u32x2{pk(q[0] * SCL_A, q[1] * SCL_A), pk(q[2] * SCL_A, q[3] * SCL_A)};
  *(LAS u32x2*)(vi + (c.g & 1) * 288 + c.c16 * 16 + (c.g >> 1) * 8) = u32x2{pk(v[0], v[1]), pk(v[2], v[3])};
  if (c.g == 0) *(LAS unsigned*)(lds + AK1 + (half * 32 + (tl >> 1)) * 128 + r * 4) = pk(e[0], e[1]);
  else if (c.g == 1) *(LAS unsigned*)(qi + 1024 + r * 4) = pk(e[0] * SCL_A, e[1] * SCL_A);
  else if (c.g == 2) {
    unsigned x = pk(e[0], e[1]);
    ldsp a = vi + ((c.c16 >> 2) & 1) * 288 + 256 + ((c.c16 >> 3) * 4 + (c.c16 & 3)) * 2;
    *(LAS unsigned short*)a = (unsigned short)x; *(LAS unsigned short*)(a + 16) = (unsigned short)(x >> 16);
  }
}
DEV int a_piece(int pc, int half) { return pc < 32 ? half * 32768 + pc * 1024 : pc < 36 ? AK1 + half * 4096 + (pc - 32) * 1024 : AV + half * 36864 + (pc - 36) * 1024; }
DEV float max16(f32x16 s, float m) {
#pragma unroll
  for (int i = 0; i < 16; i += 2) m = __builtin_fmaxf(__builtin_fmaxf(m, s[i]), s[i + 1]);
  return m;
}
DEV float x32max(float x) { auto t = __builtin_amdgcn_permlane32_swap(f2u(x), f2u(x), false, false); return fmaxf(u2f(t[0]), u2f(t[1])); }
DEV void a_pv(f32x16 s, u32x4 v0, u32x4 v1, f32x16& o) {
  float e[16];
#pragma unroll
  for (int i = 0; i < 16; ++i) e[i] = __builtin_amdgcn_exp2f(s[i]);
  o = mfma32(v0, u32x4{pk(e[0], e[1]), pk(e[2], e[3]), pk(e[4], e[5]), pk(e[6], e[7])}, o);
  o = mfma32(v1, u32x4{pk(e[8], e[9]), pk(e[10], e[11]), pk(e[12], e[13]), pk(e[14], e[15])}, o);
}
DEV void role_a(const Params& p, Ctx c, int h, int half, int job, ldsp lds) {
  relane(c);
  u32x4 Q0[8]; unsigned Q1[8];
  {
    u32x4 W[18][4];
    {
      const int dw = c.lane & 31, t = dw >> 3, e = dw & 7;
      int col = t == 0 ? 576 + 2 * e : t == 1 ? 2 * e : t == 2 ? 1152 + (e >> 2) * 4 + ((e >> 1) & 1) * 10 + (e & 1) * 2 : e == 2 ? 16 : e == 4 ? 1160 : 592;
      const char* src = p.awqkv + (c.lane >> 5) * 3456 + (h * 18 + col) * 2;
      for (int pc = c.w; pc < 288; pc += 4) __builtin_amdgcn_global_load_lds((const unsigned*)(src + pc * 6912), (LAS unsigned*)(lds + pc * 256), 4, 0, 0);
    }
    const rsrc_t rx = __builtin_amdgcn_make_buffer_rsrc((void*)p.ax, 0, 2048 * 1152, 0x00020000);
    const int vo = c.c16 * 1152 + c.g * 32, s0 = (half * 1024 + c.w * 256) * 1152;
    u32x4 X0[18], X1[18];
    a_rows<0>(rx, vo, s0, X0);
    vm0(); bar();
    a_wfrags(c, lds, W, 32, 8);
    bar();
    for (int t = 0; t < 16; t += 2) {
      PIN_A(W);
      a_rows<0>(rx, vo, s0 + (t + 1) * 18432, X1);
      __builtin_amdgcn_sched_barrier(0);
      a_proj_tile(c, W, X0, c.w * 16 + t, half, lds);
      if (t < 14) a_rows<0>(rx, vo, s0 + (t + 2) * 18432, X0);
      __builtin_amdgcn_sched_barrier(0);
      a_proj_tile(c, W, X1, c.w * 16 + t + 1, half, lds);
    }
  }
  if (c.lane == 0) *(LAS u32x4*)(lds + AONE) = u32x4{0x3F803F80u, 0x3F803F80u, 0x3F803F80u, 0x3F803F80u};
  bar();
  for (int pc = c.w; pc < 72; pc += 4) st16(*(LAS u32x4*)(lds + a_piece(pc, half) + c.lane * 16), c.ws, AKV_O + pc * 1024 + c.lane * 16, (h * 2 + half) * 73728);
#pragma unroll
  for (int i = 0; i < 8; ++i) {
    ldsp qi = lds + AV + (half ^ 1) * 36864 + (c.w * 8 + i) * 1152;
    Q0[i] = *(LAS u32x4*)(qi + c.lane * 16);
    unsigned x = *(LAS unsigned*)(qi + 1024 + (c.lane & 31) * 4);
    Q1[i] = c.lane < 32 ? x : 0u;
  }
  vm0(); bar();
  if (c.w == 0) set_flag(c, F_AKV + h * 2 + half, 0);
  wait_flags(c, F_AKV + h * 2 + (half ^ 1), 1, 0);
  for (int pc = c.w; pc < 72; pc += 4)
    __builtin_amdgcn_global_load_lds((const unsigned*)(p.ws + AKV_O + (h * 2 + (half ^ 1)) * 73728 + pc * 1024 + c.lane * 16), (LAS unsigned*)(lds + a_piece(pc, half ^ 1)), 16, 0, SC1);
  vm0(); bar();
  {
    const int r = c.lane & 31, hh = c.lane >> 5;
    ldsp k0p = lds + c.lane * 16, k1p = lds + AK1 + r * 4;
    const int vstep = r < 18 ? 576 : 0;
    ldsp vp0 = lds + (r < 18 ? AV + hh * 288 + r * 16 : AONE);
#pragma unroll
    for (int pr = 0; pr < 4; ++pr) {
      const u32x4 qa0 = Q0[2 * pr], qb0 = Q0[2 * pr + 1], qa1 = {Q1[2 * pr], 0, 0, 0}, qb1 = {Q1[2 * pr + 1], 0, 0, 0};
      f32x16 oa = {}, ob = {}, na, nb, sa, sb;
      {
        u32x4 k0 = *(LAS u32x4*)k0p; unsigned k1 = *(LAS unsigned*)k1p;
        u32x4 k1v = {k1, k1, k1, k1};
        sa = mfma32(k0, qa0, mfma32(k1v, qa1, oa)); sb = mfma32(k0, qb0, mfma32(k1v, qb1, ob));
        float ma = x32max(max16(sa, sa[0])), mb = x32max(max16(sb, sb[0]));
#pragma unroll
        for (int i = 0; i < 16; ++i) { na[i] = -ma; nb[i] = -mb; sa[i] -= ma; sb[i] -= mb; }
      }
      u32x4 k0 = *(LAS u32x4*)(k0p + 1024); unsigned k1 = *(LAS unsigned*)(k1p + 128);
      ldsp vp = vp0;
      for (int kt = 0; kt < 63; ++kt) {
        u32x4 v0 = *(LAS u32x4*)vp, v1 = *(LAS u32x4*)(vp + vstep);
        vp += 2 * vstep;
        u32x4 k1v = {k1, k1, k1, k1};
        f32x16 ta = mfma32(k0, qa0, mfma32(k1v, qa1, na)), tb = mfma32(k0, qb0, mfma32(k1v, qb1, nb));
        const int kn = kt < 62 ? kt + 2 : 63;
        k0 = *(LAS u32x4*)(k0p + kn * 1024); k1 = *(LAS unsigned*)(k1p + kn * 128);
        a_pv(sa, v0, v1, oa); a_pv(sb, v0, v1, ob);
        float ma = max16(ta, 0.f), mb = max16(tb, 0.f);
        if (__any(fmaxf(ma, mb) > 10.f)) {
          ma = x32max(ma); mb = x32max(mb);
          float fa = __builtin_amdgcn_exp2f(-ma), fb = __builtin_amdgcn_exp2f(-mb);
#pragma unroll
          for (int i = 0; i < 16; ++i) { na[i] -= ma; nb[i] -= mb; ta[i] -= ma; tb[i] -= mb; oa[i] *= fa; ob[i] *= fb; }
        }
        sa = ta; sb = tb;
      }
      {
        u32x4 v0 = *(LAS u32x4*)vp, v1 = *(LAS u32x4*)(vp + vstep);
        a_pv(sa, v0, v1, oa); a_pv(sb, v0, v1, ob);
      }
      const int oo = AO_O + (half * 1024 + c.w * 256 + pr * 64 + r) * 1152 + h * 36;
      const float ia = __builtin_amdgcn_rcpf(oa[10]), ib = __builtin_amdgcn_rcpf(ob[10]);
      st16(u32x4{pk(oa[0] * ia, oa[1] * ia), pk(oa[2] * ia, oa[3] * ia), pk(oa[4] * ia, oa[5] * ia), pk(oa[6] * ia, oa[7] * ia)}, c.ws, oo + hh * 20, 0);
      st16(u32x4{pk(ob[0] * ib, ob[1] * ib), pk(ob[2] * ib, ob[3] * ib), pk(ob[4] * ib, ob[5] * ib), pk(ob[6] * ib, ob[7] * ib)}, c.ws, oo + hh * 20, 32 * 1152);
      if (hh == 0) { st4(pk(oa[8] * ia, oa[9] * ia), c.ws, oo + 16, 0); st4(pk(ob[8] * ib, ob[9] * ib), c.ws, oo + 16, 32 * 1152); }
    }
  }
  vm0(); bar();
  if (c.w == 0) set_flag(c, F_AO + h * 2 + half, 0);
  if (job >= 63) return;
  relane(c);
  const int cg = job % 9, tg = job / 9, t0 = tg * 18 + (tg < 2 ? tg : 2), t1 = t0 + (tg < 2 ? 19 : 18);
  for (int pc = c.w; pc < 72; pc += 4) dma16(p.awo + (pc * 8 + (c.lane >> 3)) * 1152 + cg * 128 + (c.lane & 7) * 16, lds + pc * 1024);
  vm0(); bar();
  u32x4 W[18][4];
  a_wfrags(c, lds, W, 8, 32);
  wait_flags(c, F_AO, 64, 0);
  const int vo = AO_O + c.c16 * 1152 + c.g * 32;
  for (int t = t0 + c.w; t < t1; t += 4) {
    PIN_A(W);
    u32x4 X[18];
    a_rows<SC1>(c.ws, vo, t * 18432, X);
    f32x4 d[4] = {};
#pragma unroll
    for (int ks = 0; ks < 18; ++ks)
#pragma unroll
      for (int i = 0; i < 4; ++i) d[i] = mfma16(W[ks][i], X[ks], d[i]);
    char* o = p.out + (size_t)(t * 16 + c.c16) * 2304 + cg * 128 + c.g * 32;
    *(u32x4*)o = pk8(d[0], d[1]); *(u32x4*)(o + 16) = pk8(d[2], d[3]);
  }
}

__global__ void __launch_bounds__(256, 1) fused_kernel(Params p) {
  extern __shared__ __attribute__((aligned(16))) char lds_raw[];
  ldsp lds = (ldsp)lds_raw;
  Ctx c;
  c.ws = __builtin_amdgcn_make_buffer_rsrc((void*)p.ws, 0, WS_END, 0x00020000);
  c.t0 = rt32();
  c.w = __builtin_amdgcn_readfirstlane(threadIdx.x >> 6); c.lane = c.g = c.c16 = 0;
  const int x = blockIdx.x & 7, r = blockIdx.x >> 3;
  if (x < 4) {
    if (r < 8) { role_score(p, c, x * 8 + r, lds); return; }
    if (r < 26) { int j = x * 18 + r - 8; job_c(p, c, j, lds); role_mixer(p, c, j, lds); return; }
  } else {
    if (r < 8) { role_qk(p, c, (x - 4) * 8 + r, lds); return; }
    if (r < 16) { role_vu(p, c, (x - 4) * 8 + r - 8, lds); return; }
  }
  if (x >= 4) {
    const int hl = r - 16;
    if (hl < 8) job_c(p, c, 96 + (x - 4) * 8 + hl, lds);
    role_a(p, c, (x & 1) * 16 + hl, (x >> 1) & 1, (x - 4) * 16 + hl, lds);
    return;
  }
  const int gi = x * 6 + (r - 26);
  job_c(p, c, 72 + gi, lds);
  for (int job = gi; job < 384; job += 24) job_out(p, c, job / 6, job % 6, lds);
}

extern "C" void kernel_launch(void* const* d_in, const int* in_sizes, int n_in, void* d_out, int out_size, void* d_ws, size_t ws_size,
                              hipStream_t stream) {
  Params p;
  p.ax = (const char*)d_in[0]; p.awqkv = (const char*)d_in[1]; p.awo = (const char*)d_in[2];
  p.bx = (const char*)d_in[3]; p.mem0 = (const char*)d_in[4];
  p.wq = (const char*)d_in[5]; p.wk = (const char*)d_in[6]; p.wv = (const char*)d_in[7]; p.wo = (const char*)d_in[8];
  p.out = (char*)d_out;
  p.ws = (char*)(((uintptr_t)d_ws + ((2u << 20) - 1)) & ~(uintptr_t)((2u << 20) - 1));
  (void)hipFuncSetAttribute((const void*)fused_kernel, hipFuncAttributeMaxDynamicSharedMemorySize, LDS_BYTES);
  (void)hipMemsetAsync(p.ws, 0xFF, INIT_BYTES, stream);
  hipLaunchKernelGGL(fused_kernel, dim3(256), dim3(256), LDS_BYTES, stream, p);
}
```

```cpp
#include <hip/hip_runtime.h>
#include <stdint.h>

typedef __bf16 bf16x2 __attribute__((ext_vector_type(2)));
typedef __bf16 bf16x8 __attribute__((ext_vector_type(8)));
typedef __bf16 bf16x16 __attribute__((ext_vector_type(16)));
typedef float f32x2 __attribute__((ext_vector_type(2)));
typedef float f32x4 __attribute__((ext_vector_type(4)));
typedef float f32x16 __attribute__((ext_vector_type(16)));
typedef unsigned u32x2 __attribute__((ext_vector_type(2)));
typedef unsigned u32x4 __attribute__((ext_vector_type(4)));
typedef unsigned u32x8 __attribute__((ext_vector_type(8)));
typedef short s16x4 __attribute__((ext_vector_type(4)));
#define LAS __attribute__((address_space(3)))
#define DEV __device__ __forceinline__
typedef LAS char* ldsp;
typedef __amdgpu_buffer_rsrc_t rsrc_t;

constexpr int RING = 8;
constexpr int SLOT_PH = 65536;
constexpr int P_O = 0, MA_O = 20480;
constexpr int ST_O = RING * SLOT_PH;
constexpr int FL = ST_O + 524288;
constexpr int F_KX = 0, F_U = 32, F_PX = 64, F_Y = 128, F_POL = 200, F_AKV = 208, F_AO = 272, F_C = 336, F_END = 464;
constexpr int INIT_BYTES = FL + F_END * 128;
constexpr int C_O = 2 << 20;
constexpr int Z_O = C_O + 72 * 294912;
constexpr int KX_O = Z_O + 64 * 72 * 20480;
constexpr int PX_O = KX_O + 64 * 32 * 16384;
constexpr int AKV_O = PX_O + 64 * 32 * 2560;
constexpr int AO_O = AKV_O + 64 * 75776;
constexpr int DBG_O = AO_O + 2048 * 1152;
constexpr int WS_END = DBG_O + (1 << 20);
constexpr int LDS_BYTES = 160 * 1024;
constexpr int SC1 = 16;
constexpr int CODE_LINES = 700;
constexpr int OOB = 0x7FFFFFF0;

struct Params {
  const char *ax, *awqkv, *awo, *bx, *mem0, *wq, *wk, *wv, *wo;
  char* out; char* ws;
};

DEV unsigned pk(float a, float b) { f32x2 v = {a, b}; return __builtin_bit_cast(unsigned, __builtin_convertvector(v, bf16x2)); }
DEV u32x4 pk8(f32x4 a, f32x4 b) { return u32x4{pk(a[0], a[1]), pk(a[2], a[3]), pk(b[0], b[1]), pk(b[2], b[3])}; }
DEV unsigned umin_(unsigned a, unsigned b) { return a < b ? a : b; }
DEV unsigned umax_(unsigned a, unsigned b) { return a > b ? a : b; }
DEV bf16x8 as8(u32x4 v) { return __builtin_bit_cast(bf16x8, v); }
DEV f32x4 mfma16(u32x4 a, u32x4 b, f32x4 c) { return __builtin_amdgcn_mfma_f32_16x16x32_bf16(as8(a), as8(b), c, 0, 0, 0); }
DEV f32x16 mfma32(u32x4 a, u32x4 b, f32x16 c) { return __builtin_amdgcn_mfma_f32_32x32x16_bf16(as8(a), as8(b), c, 0, 0, 0); }
DEV f32x4 smf(u32x4 a, u32x8 b, f32x4 c, int idx) { return __builtin_amdgcn_smfmac_f32_16x16x64_bf16(as8(a), __builtin_bit_cast(bf16x16, b), c, idx, 0, 0); }
#define MFMA16_ACC(d, a, b) asm volatile("v_mfma_f32_16x16x32_bf16 %0, %1, %2, %0" : "+v"(d) : "v"(a), "v"(b))
#define MFMA16_Z(d, a, b) asm volatile("v_mfma_f32_16x16x32_bf16 %0, %1, %2, 0" : "=&v"(d) : "v"(a), "v"(b))
#define MFMA16_C(d, a, b, c) asm volatile("v_mfma_f32_16x16x32_bf16 %0, %1, %2, %3" : "=&v"(d) : "v"(a), "v"(b), "v"(c))
#define MFMA32_ACC(d, a, b) asm volatile("v_mfma_f32_32x32x16_bf16 %0, %1, %2, %0" : "+v"(d) : "v"(a), "v"(b))
#define MFMA32_C(d, a, b, c) asm volatile("v_mfma_f32_32x32x16_bf16 %0, %1, %2, %3" : "=&v"(d) : "v"(a), "v"(b), "v"(c))
#define SB() __builtin_amdgcn_sched_barrier(0)
DEV u32x2 trd(ldsp p) { return __builtin_bit_cast(u32x2, __builtin_amdgcn_ds_read_tr16_b64_v4i16((LAS s16x4*)p)); }
DEV u32x4 ld16(rsrc_t r, int vo, int so) { return __builtin_amdgcn_raw_buffer_load_b128(r, vo, so, SC1); }
DEV u32x2 ld8(rsrc_t r, int vo, int so) { return __builtin_amdgcn_raw_buffer_load_b64(r, vo, so, SC1); }
DEV unsigned ld4(rsrc_t r, int vo, int so) { return __builtin_amdgcn_raw_buffer_load_b32(r, vo, so, SC1); }
DEV void st16(u32x4 v, rsrc_t r, int vo, int so) { __builtin_amdgcn_raw_buffer_store_b128(v, r, vo, so, SC1); }
DEV void st8(u32x2 v, rsrc_t r, int vo, int so) { __builtin_amdgcn_raw_buffer_store_b64(v, r, vo, so, SC1); }
DEV void st4(unsigned v, rsrc_t r, int vo, int so) { __builtin_amdgcn_raw_buffer_store_b32(v, r, vo, so, SC1); }
DEV void dma16(const char* g, ldsp l) { __builtin_amdgcn_global_load_lds((const unsigned*)g, (LAS unsigned*)l, 16, 0, 0); }
DEV void vm0() { asm volatile("s_waitcnt vmcnt(0)" ::: "memory"); }
DEV void bar() { asm volatile("s_waitcnt lgkmcnt(0)\n\ts_barrier" ::: "memory"); }
DEV unsigned rt32() { return (unsigned)__builtin_amdgcn_s_memrealtime(); }
DEV int mapo(int o) { return ((o >> 13) << 14) + (o & 8191); }
template <int CTRL> DEV float dpp(float x) { return __builtin_bit_cast(float, __builtin_amdgcn_update_dpp(0, __builtin_bit_cast(int, x), CTRL, 0xf, 0xf, true)); }
DEV float u2f(unsigned x) { return __builtin_bit_cast(float, x); }
DEV unsigned f2u(float x) { return __builtin_bit_cast(unsigned, x); }
DEV float vmax(float a, float b) { float r; asm("v_max_f32 %0, %1, %2" : "=v"(r) : "v"(a), "v"(b)); return r; }
DEV float vmax3(float a, float b, float c) { float r; asm("v_max3_f32 %0, %1, %2, %3" : "=v"(r) : "v"(a), "v"(b), "v"(c)); return r; }
DEV float xmax(float x) {
  auto s = __builtin_amdgcn_permlane16_swap(f2u(x), f2u(x), false, false); x = vmax(u2f(s[0]), u2f(s[1]));
  auto t = __builtin_amdgcn_permlane32_swap(f2u(x), f2u(x), false, false); return vmax(u2f(t[0]), u2f(t[1]));
}
DEV float x32sum(float x) { auto t = __builtin_amdgcn_permlane32_swap(f2u(x), f2u(x), false, false); return u2f(t[0]) + u2f(t[1]); }
DEV float x16sum(float x) { auto t = __builtin_amdgcn_permlane16_swap(f2u(x), f2u(x), false, false); return u2f(t[0]) + u2f(t[1]); }
DEV float xsum(float x) { return x32sum(x16sum(x)); }
DEV float fold32(float a, float b) { auto t = __builtin_amdgcn_permlane32_swap(f2u(a), f2u(b), false, false); return u2f(t[0]) + u2f(t[1]); }

struct Ctx { rsrc_t ws; unsigned t0; int lane, w, g, c16; };
DEV void relane(Ctx& c) {
  int l; asm volatile("v_mbcnt_lo_u32_b32 %0, -1, 0\n\tv_mbcnt_hi_u32_b32 %0, -1, %0" : "=v"(l));
  c.lane = l; c.g = l >> 4; c.c16 = l & 15;
}
#define DBGV(wv, i, v) do {} while (0)
#define DBGCNT() do {} while (0)
#define DBGEND(wv) do {} while (0)
#define DBGW(i, v) do {} while (0)
#define STAMPC(i) do {} while (0)
#define STAMP(i) do {} while (0)
DEV bool expired(const Ctx& c) { return rt32() - c.t0 > 3000000u; }
DEV void wait_flags(const Ctx& c, int f0, int n, int v, int f1 = 0, int n1 = 0) {
  int vo = FL + (c.lane < n ? f0 + c.lane : c.lane < n + n1 ? f1 + c.lane - n : f0) * 128;
  for (;;) {
    int x = (int)ld4(c.ws, vo, 0);
    if (__all(x >= v)) break;
    if (expired(c)) break;
    __builtin_amdgcn_s_sleep(4);
  }
  asm volatile("" ::: "memory");
}
DEV int peek_flags(const Ctx& c, int f0, int n, int v, int known) { return (int)ld4(c.ws, known >= v ? OOB : FL + (f0 + (c.lane < n ? c.lane : 0)) * 128, 0); }
DEV void wait_flags_k(const Ctx& c, int f0, int n, int v, int& known, int x) {
  if (__builtin_expect(known >= v, 1)) return;
  for (;;) {
    if (__all(x >= v)) { known = __all(x >= v + 8) ? v + 8 : __all(x >= v + 3) ? v + 3 : v; break; }
    if (expired(c)) break;
    x = peek_flags(c, f0, n, v, known);
    __builtin_amdgcn_s_sleep(2);
  }
  asm volatile("" ::: "memory");
}
#define G_M 80
#define G_MMIN 10
#define G_P 160
#define G_PMIN 110
#define D_MX 40
#define ZSL 0
#define ZPOS -2
#define FDEL 1600u
#define TRM 300
#define TRP 240
#define GPO 120
#define TLA 180
#define TPO 100
#define CPUB 8
#define ADEL 1900u
#define KXSL 12
constexpr int T_RETM = TRM, T_RETP = TRP, T_POLL = TPO, G_POLL = GPO, T_LAT = TLA, T_CAL = 12;
struct Clk { unsigned base; int t; unsigned m; float k; int rm, rp, ds, dm; bool ok; };
DEV int now_t(const Clk& q) { return (int)((rt32() * 4 - q.base) & 0x7FFFFFFF); }
DEV int wait_until(const Clk& q, int T) {
  int now = now_t(q), rem = T - now;
  if (rem > 40000 || rem < 0) return now + 16;
  while (rem >= 22) rem = T - now_t(q);
  for (; rem > 8; rem -= 4) asm volatile("s_nop 1");
  return T;
}
DEV unsigned cyc() { return (unsigned)__builtin_amdgcn_s_memtime(); }
DEV void clk_set(Clk& q, int t, unsigned m) {
  q.k = __builtin_fminf(__builtin_fmaxf((float)(t - q.t) * __builtin_amdgcn_rcpf((float)(int)(m - q.m)), 0.16f), 0.22f);
  q.t = t; q.m = m;
}
DEV int clk_at(const Clk& q, unsigned m) { return q.t + (int)((float)(int)(m - q.m) * q.k); }
#define MAXDPP(m, ctl) asm volatile("s_nop 1\n\tv_max_u32_dpp %0, %0, %0 " ctl " row_mask:0xf bank_mask:0xf" : "+v"(m))
template <bool WIDE> DEV int latest(u32x4 v) {
  unsigned m = umax_(umax_(umax_(v.x, v.y), v.z), v.w);
  MAXDPP(m, "row_ror:1"); MAXDPP(m, "row_ror:2"); MAXDPP(m, "row_ror:4");
  if (!WIDE) return __builtin_amdgcn_readfirstlane((int)m);
  MAXDPP(m, "row_ror:8");
  return (int)umax_((unsigned)__builtin_amdgcn_readlane((int)m, 0), (unsigned)__builtin_amdgcn_readlane((int)m, 16));
}
template <bool WIDE> DEV int poll_stamps(const Ctx& c, const Clk& q, int vo, int so, int& t) {
  int v;
  for (int u = t;; u += T_POLL) {
    u = wait_until(q, u);
    v = latest<WIDE>(ld16(c.ws, vo, so));
    if (v != -1 || expired(c)) break;
  }
  t = (v + G_POLL <= t ? t : v + G_POLL <= t + T_POLL ? t + T_POLL : v + G_POLL + T_POLL) + T_LAT;
  return v;
}
DEV int adapted(int d, int need) { return max(min(need, d + 12), d - (max(d - need, 0) >> 3)); }
DEV void plan_p(const Ctx& c, Clk& q, int n, int tm, int sm, int sp) {
  int t = q.rm + q.ds;
  if (n == 0) tm = 0;
  if (n < 60) DBGW(18 + n * 4, tm == -1);
  if (tm == -1) { int u = q.rm + 400; tm = poll_stamps<true>(c, q, sm, mapo((n - 1) * 4096), u); }
  q.dm = adapted(q.dm, q.ok ? tm + G_M - q.rp : q.dm);
  q.ok = tm + G_MMIN <= q.rm && n > 0;
  if (!q.ok) {
    t += T_RETM;
    if (tm + G_MMIN > q.rm + T_RETM - 60 || n == 0) { t += G_POLL - G_P; poll_stamps<false>(c, q, sp, mapo(n * 4096), t); }
  }
  q.rp = t;
}
DEV void plan_m(const Ctx& c, Clk& q, int n, int tp, int sm, int sp) {
  int t = q.rp + q.dm;
  if (n < 60) DBGW(19 + n * 4, tp == -1);
  if (tp == -1) { int u = q.rp + 200; tp = poll_stamps<false>(c, q, sp, mapo(n * 4096), u); }
  q.ds = adapted(q.ds, q.ok ? tp + G_P - q.rm : q.ds);
  q.ok = tp + G_PMIN <= q.rp && n > 0;
  if (!q.ok) {
    t += T_RETP;
    if (tp + G_PMIN > q.rp + T_RETP - 60 || n == 0) { t += G_POLL - G_M; poll_stamps<true>(c, q, sm, mapo(n * 4096), t); }
  }
  q.rm = t;
}
DEV void set_flag(const Ctx& c, int f, int v) { if (c.lane == 0) st4((unsigned)v, c.ws, FL + f * 128, 0); }

DEV int swz3(int row) { return (row & 1) | ((((row >> 1) ^ (row >> 2)) & 1) << 1) | (((row >> 3) & 1) << 2); }
DEV void stage_w(const Ctx& c, const char* W, int h, int d0, int nrows, ldsp img, int rot = 0) {
  for (int i = c.w + rot; i < nrows / 4 + rot; i += 4) {
    const int p = i < nrows / 4 ? i : i - nrows / 4;
    int row = p * 4 + c.g;
    int ch = c.c16 ^ (swz3(row) << 1);
    dma16(W + (size_t)(d0 + row) * 8192 + h * 256 + ch * 16, img + p * 1024);
  }
}
DEV ldsp w_addr(ldsp img, int row, int e0, int p) { return img + row * 256 + ((((e0 >> 4) ^ swz3(row)) << 5) | (p << 3)); }

struct XSt { rsrc_t r; int vo; unsigned xe; ldsp dst; int pf; };
DEV XSt x_init(const Ctx& c, const char* X, ldsp lds, int k) {
  const int ln = (k * 4 + c.w) * 5 + c.lane;
  return XSt{__builtin_amdgcn_make_buffer_rsrc((void*)X, 0, 2048 * 1152, 0x00020000), (c.w * 8 + (c.lane >> 3)) * 1152 + (((c.lane & 7) ^ (c.w * 2 + (c.g & 1))) << 4),
             (unsigned)(size_t)lds + (c.c16 >> 2) * 1024 + (c.c16 & 3) * 128 + ((c.g ^ (c.c16 >> 1)) << 4), lds + c.w * 1024, c.lane < 5 && ln < 288 ? ln * 128 : OOB};
}
DEV void x_touch(const XSt& x, int s) { __builtin_amdgcn_raw_ptr_buffer_load_lds(x.r, x.dst + 155648, 4, x.pf, min(s, 63) * 36864, 0, 0); }
DEV void x_piece(const XSt& x, int s, int bo, int i) { __builtin_amdgcn_raw_ptr_buffer_load_lds(x.r, x.dst + bo + i * 4096, 16, x.vo, min(s, 63) * 36864 + i * 128, 0, 0); }
#define X_READ(b, xa, ks) asm volatile("ds_read_b128 %0, %2 offset:%3\n\tds_read_b128 %1, %2 offset:%4" : "=&v"(b[0]), "=&v"(b[1]) : "v"(xa[(ks) & 1]), "i"(((ks) >> 1) * 4096), "i"(((ks) >> 1) * 4096 + 512) : "memory")
#define LGKM(n, b) asm volatile("s_waitcnt lgkmcnt(" #n ")" : "+v"(b[0]), "+v"(b[1]) :: "memory")
#define LDSW(op, a, v, o) asm volatile(op " %0, %1 offset:%2" :: "v"(a), "v"(v), "i"(o) : "memory")
DEV int x_next(int bo) { return bo == 73728 ? 0 : bo + 36864; }

#define PIN_W(W) do { _Pragma("unroll") for (int t_ = 0; t_ < 7; ++t_) _Pragma("unroll") for (int i_ = 0; i_ < 4; ++i_) asm volatile("" : "+a"(W[t_][i_])); } while (0)

template <int PUP, class F> DEV void project_m(const Ctx& c, int vo0, int vo8, int so, u32x8 (&W)[9][4], f32x4 (&acc)[4], int n, F hook) {
  int dbgcnt = 0;
  const int idx = (c.lane & 8) ? 0xEEEE : 0x4444;
  for (;;) {
    u32x4 m[9];
#pragma unroll
    for (int t = 0; t < PUP; ++t) {
      m[t] = ld16(c.ws, t < 4 ? vo0 : vo8, so + t * 1024);
      if (t < 4) { acc[t] = f32x4{0.f, 0.f, 0.f, 0.f}; asm volatile("s_nop 2" : "+v"(acc[t])); } else asm volatile("s_nop 4");
      SB();
    }
    unsigned mx = 0;
#pragma unroll
    for (int t = 0; t < 9; ++t) {
      if (t < 9 - PUP) { m[PUP + t] = ld16(c.ws, vo8, so + (PUP + t) * 1024); SB(); }
      hook(t); SB();
#pragma unroll
      for (int i = 0; i < 4; ++i) acc[i] = smf(m[t], W[t][i], acc[i], idx);
      mx = umax_(umax_(mx, m[t].x), m[t].y); mx = umax_(umax_(mx, m[t].z), m[t].w);
      asm volatile("" : "+v"(mx));
      SB();
    }
    hook(9); SB();
    if (!__any(mx == 0xFFFFFFFFu)) break;
    if (expired(c)) break;
    DBGCNT();
  }
  hook(dbgcnt + 1000);
}

template <class F> DEV void project_l(const Ctx& c, int vo0, int vo8, int so, u32x8 (&W)[9][4], f32x4 (&acc)[4], ldsp mb, F hook) {
  int dbgcnt = 0;
  const int idx = (c.lane & 8) ? 0xEEEE : 0x4444;
  for (bool first = true;; first = false) {
    __builtin_amdgcn_raw_ptr_buffer_load_lds(c.ws, mb + c.w * 1024, 16, vo0, so + c.w * 1024, 0, SC1);
    __builtin_amdgcn_raw_ptr_buffer_load_lds(c.ws, mb + 4096 + c.w * 1024, 16, vo8, so + 4096 + c.w * 1024, 0, SC1);
    if (c.w == 2) __builtin_amdgcn_raw_ptr_buffer_load_lds(c.ws, mb + 8192, 16, vo8, so + 8192, 0, SC1);
    vm0(); bar();
    u32x4 m[9];
#pragma unroll
    for (int t = 0; t < 9; ++t) m[t] = *(LAS u32x4*)(mb + t * 1024 + c.lane * 16);
#pragma unroll
    for (int t = 0; t < 4; ++t) { acc[t] = f32x4{0.f, 0.f, 0.f, 0.f}; asm volatile("" : "+v"(acc[t])); }
    SB();
    unsigned mx = 0;
#pragma unroll
    for (int t = 0; t < 9; ++t) {
      hook(t, first); SB();
#pragma unroll
      for (int i = 0; i < 4; ++i) acc[i] = smf(m[t], W[t][i], acc[i], idx);
      mx = umax_(umax_(mx, m[t].x), m[t].y); mx = umax_(umax_(mx, m[t].z), m[t].w);
      asm volatile("" : "+v"(mx));
      SB();
    }
    hook(9, first); SB();
    if (__builtin_expect(!__any(mx == 0xFFFFFFFFu), 1)) break;
    bool ex = expired(c);
    if (c.lane == 0) *(LAS unsigned*)(mb + 9216 + c.w * 4) = ex;
    bar();
    u32x4 e4 = *(LAS u32x4*)(mb + 9216);
    if (__builtin_amdgcn_readfirstlane(e4.x | e4.y | e4.z | e4.w)) break;
    DBGCNT();
  }
  hook(dbgcnt + 1000, false);
}

constexpr float SCL_B = 0.08838834764831845f * 1.4426950408889634f;
DEV float merge8(float lo, float hi) {
  return u2f((unsigned)__builtin_amdgcn_update_dpp((int)f2u(lo), (int)f2u(hi), 0x128, 0xf, 0xc, false));
}
DEV int get_pol(const Ctx& c, unsigned& base) {
  wait_flags(c, F_POL, 1, 0);
  base = (unsigned)__builtin_amdgcn_readfirstlane((int)ld4(c.ws, FL + F_POL * 128, 0));
  return (base & 1) * 8192;
}

template <int KIND> DEV void score_loop(const Ctx& c, int h, u32x8 (&W)[9][4], ldsp lds) {
  Clk ck = {0, 0, cyc(), 0.f, 0, 0, 480, 330, false};
  const int po = get_pol(c, ck.base);
  const int ma0 = mapo(MA_O + c.lane * 16) + po, ma8 = mapo(MA_O + 4096 + c.lane * 16) - 4096 + po;
  const int r8 = c.c16 & 7, blk = c.c16 >> 3, tq = c.w & 1;
  const int kxo = KX_O + h * 16384 + c.lane * 16;
  int o1, o2;
  if (KIND == 0) { o1 = mapo(P_O + h * 640 + 128 + c.g * 128 + r8 * 16 + blk * 8) + po; o2 = mapo(P_O + h * 640 + r8 * 16 + (c.g & 1) * 8 + blk * 4) + po; }
  else if (KIND == 1) { o1 = mapo(P_O + h * 640 + (c.lane < 40 ? c.lane : 0) * 16) + po; o2 = c.lane < 18 ? ST_O + po + 1024 + c.lane * 128 : OOB; }
  else { o1 = PX_O + h * 2560 + tq * 1280 + c.lane * 16; o2 = PX_O + h * 2560 + tq * 1280 + 1024 + c.c16 * 16 + (c.g & 1) * 8; }
  const float cmv = c.g < 2 ? -1e30f : 0.f;
  f32x4 cm = {cmv, cmv, cmv, cmv};
  u32x4 kx[8], qx[4];
  int known = -1, fx = -1;
  const int tokq = 8 * (c.c16 >> 2) + 4 * tq + (c.c16 & 3) - 8 * c.g;
  ldsp wa = lds + (c.w * 32 + c.c16) * 32 + ((c.g ^ ((c.c16 >> 2) & 3)) << 3);
  ldsp ra = lds + (c.lane >> 2) * 32 + (((c.lane & 3) ^ c.g) << 3);
  for (int n = 0; n < 64; ++n) {
    PIN_W(W);
    f32x4 x1, x2, mm, acc[4];
    if (KIND != 1) asm volatile("" : "+v"(cm));
    if (KIND != 1) {
      if (KXSL) __builtin_amdgcn_s_sleep(KIND == 0 ? KXSL : KXSL - 2);
      wait_flags_k(c, F_KX + h, 1, n, known, fx);
#pragma unroll
      for (int i = 0; i < 8; ++i) kx[i] = ld16(c.ws, kxo, n * 524288 + i * 1024);
      if (KIND == 2) {
#pragma unroll
        for (int i = 0; i < 4; ++i) qx[i] = ld16(c.ws, kxo, n * 524288 + 8192 + tq * 4096 + i * 1024);
      }
    }
    int tb = 0; unsigned mb = 0;
    if (KIND == 1) { tb = wait_until(ck, ck.rm - T_CAL); mb = cyc(); }
    bar();
    u32x4 stv; int dbgp = 0;
    project_m<9>(c, ma0, ma8, ((n - 1) & (RING - 1)) * SLOT_PH, W, acc, n, [&](int t) {
      if (KIND == 1 && t == 6) stv = ld16(c.ws, o2, mapo(((n - 1) & 63) * 4096));
      if (KIND != 1 && t == 6) fx = peek_flags(c, F_KX + h, 1, n + 1, known);
      if (t >= 1000) dbgp = t - 1000;
    });
#pragma unroll
    for (int i = 0; i < 2; ++i)
      *(LAS u32x2*)(wa + i * 512) = u32x2{pk(fold32(acc[i][0], acc[2 + i][0]), fold32(acc[i][1], acc[2 + i][1])), pk(fold32(acc[i][2], acc[2 + i][2]), fold32(acc[i][3], acc[2 + i][3]))};
    bar();
    if (KIND != 2 && n == 63) break;
    if (KIND == 1) {
      unsigned me = cyc();
      clk_set(ck, tb, mb);
      int ts = clk_at(ck, me), tm = latest<true>(stv);
      SB();
      if (c.lane == 0) st4((unsigned)ts, c.ws, ST_O + po + (h >> 2) * 128 + (h & 3) * 4, mapo(n * 4096));
      DBGV(1, 515 + n * 4, ck.rm); DBGW(768 + n * 4 + c.w, dbgp);
      plan_p(c, ck, n, tm, o2, ST_O + po + r8 * 128);
      wait_until(ck, ck.rp);
      plan_m(c, ck, n, latest<false>(ld16(c.ws, ST_O + po + r8 * 128, mapo(n * 4096))), o2, ST_O + po + r8 * 128);
      if (c.lane < 40) st16(u32x4{~0u, ~0u, ~0u, ~0u}, c.ws, o1, ((n + RING / 2) & (RING - 1)) * SLOT_PH);
      DBGV(1, 512 + n * 4, ck.ds); DBGV(1, 513 + n * 4, ck.dm); DBGV(1, 514 + n * 4, ck.rp);
      continue;
    }
    u32x4 F[4];
#pragma unroll
    for (int ks = 0; ks < 4; ++ks) { u32x2 a = trd(ra + ks * 1024), b = trd(ra + ks * 1024 + 512); F[ks] = u32x4{a.x, a.y, b.x, b.y}; }
    if (KIND == 0) {
      MFMA16_Z(x1, kx[0], F[0]); MFMA16_Z(x2, kx[4], F[0]); MFMA16_C(mm, F[0], F[0], cm);
#pragma unroll
      for (int ks = 1; ks < 4; ++ks) { MFMA16_ACC(x1, kx[ks], F[ks]); MFMA16_ACC(x2, kx[4 + ks], F[ks]); MFMA16_ACC(mm, F[ks], F[ks]); }
      asm volatile("s_nop 7" : "+v"(x1), "+v"(x2), "+v"(mm));
      float s[6];
#pragma unroll
      for (int r = 0; r < 4; ++r) s[r] = merge8(x1[r], x2[r]);
      s[4] = merge8(mm[0], mm[2]); s[5] = merge8(mm[1], mm[3]);
      float mx = vmax(vmax3(s[0], s[1], s[2]), vmax3(s[3], s[4], s[5]));
      mx = xmax(vmax(mx, dpp<0x128>(mx))) * SCL_B;
#pragma unroll
      for (int r = 0; r < 6; ++r) s[r] = __builtin_amdgcn_exp2f(s[r] * SCL_B - mx);
      float sum = ((s[0] + s[1]) + (s[2] + s[3])) + (s[4] + s[5]);
      float inv = __builtin_amdgcn_rcpf(xsum(sum + dpp<0x128>(sum)));
      const int so = (n & (RING - 1)) * SLOT_PH;
      asm volatile("" : "+v"(fx));
      st8(u32x2{pk(s[0] * inv, s[1] * inv), pk(s[2] * inv, s[3] * inv)}, c.ws, o1, so);
      if (c.g >= 2) st4(pk(s[4] * inv, s[5] * inv), c.ws, o2, so);
      DBGW(768 + n * 4 + c.w, dbgp);
    } else {
      asm volatile("" : "+v"(fx));
      set_flag(c, F_PX + h * 2 + tq, n - 1);
      MFMA16_Z(x1, kx[0], qx[0]); MFMA16_Z(x2, kx[4], qx[0]); MFMA16_C(mm, F[0], qx[0], cm);
#pragma unroll
      for (int ks = 1; ks < 4; ++ks) { MFMA16_ACC(x1, kx[ks], qx[ks]); MFMA16_ACC(x2, kx[4 + ks], qx[ks]); MFMA16_ACC(mm, F[ks], qx[ks]); }
      asm volatile("s_nop 7" : "+v"(x1), "+v"(x2), "+v"(mm));
      float s[12];
#pragma unroll
      for (int r = 0; r < 4; ++r) {
        s[r] = r <= tokq ? x1[r] * SCL_B : -1.4426950408889634e9f; s[4 + r] = r + 4 <= tokq ? x2[r] * SCL_B : -1.4426950408889634e9f;
        s[8 + r] = mm[r] * SCL_B;
      }
      float mx = vmax(vmax3(vmax3(s[0], s[1], s[2]), s[3], s[4]), vmax3(s[5], s[6], s[7]));
      mx = xmax(vmax3(vmax3(mx, s[8], s[9]), s[10], s[11]));
#pragma unroll
      for (int r = 0; r < 12; ++r) s[r] = __builtin_amdgcn_exp2f(s[r] - mx);
      float sum = ((s[0] + s[1]) + (s[2] + s[3])) + ((s[4] + s[5]) + (s[6] + s[7])) + ((s[8] + s[9]) + (s[10] + s[11]));
      float inv = __builtin_amdgcn_rcpf(xsum(sum));
      st16(u32x4{pk(s[0] * inv, s[1] * inv), pk(s[2] * inv, s[3] * inv), pk(s[4] * inv, s[5] * inv), pk(s[6] * inv, s[7] * inv)}, c.ws, o1, n * 81920);
      if (c.g >= 2) st8(u32x2{pk(s[8] * inv, s[9] * inv), pk(s[10] * inv, s[11] * inv)}, c.ws, o2, n * 81920);
      DBGW(768 + n * 4 + c.w, dbgp);
    }
  }
  if (KIND == 2) { vm0(); set_flag(c, F_PX + h * 2 + tq, 63); DBGEND(2); }
}

DEV void probe_pol(Ctx c) {
  relane(c);
  {
    unsigned best[2];
#pragma unroll
    for (int pol = 0; pol < 2; ++pol) {
      unsigned b = 0xFFFFFFFFu;
      for (int k = 0; k < 8; ++k) {
        unsigned long long a = __builtin_amdgcn_s_memtime();
        unsigned v = ld4(c.ws, mapo(31744 + (k >> 1) * 128) + pol * 8192 + (k & 1) * SLOT_PH, 0);
        asm volatile("s_waitcnt vmcnt(0)" :: "v"(v) : "memory");
        unsigned d = (unsigned)(__builtin_amdgcn_s_memtime() - a);
        b = umin_(b, d);
      }
      best[pol] = b;
    }
    set_flag(c, F_POL, (int)(((c.t0 * 4 - 400) & 0x7FFFFFFE) | (best[1] < best[0] ? 1 : 0)));
  }
}
template <int KIND> DEV void role_score(const Params& p, Ctx c, int h, ldsp lds) {
  relane(c);
  u32x8 W[9][4];
#pragma unroll
  for (int r = 0; r < 3; ++r) {
    stage_w(c, p.wq, h, r * 192, 192, lds);
    stage_w(c, p.wk, h, r * 192, 192, lds + 49152);
    vm0();
    bar();
    STAMP(13 + r);
    const int q = (c.lane >> 2) & 3, pp = c.lane & 3;
#pragma unroll
    for (int t = 0; t < 3; ++t)
#pragma unroll
      for (int i = 0; i < 4; ++i) {
        u32x8 v;
#pragma unroll
        for (int k = 0; k < 4; ++k) {
          int row = t * 64 + (k >> 1) * 32 + 16 * (c.g >> 1) + 4 * (c.g & 1) + (k & 1) * 2 + (q & 1) + (q >> 1) * 8;
          u32x2 x = trd(w_addr(lds + (i >> 1) * 49152, row, c.w * 32 + (i & 1) * 16, pp));
          v[2 * k] = x.x; v[2 * k + 1] = x.y;
        }
        if (r * 3 + t < 7) asm volatile("" : "+a"(v));
        W[r * 3 + t][i] = v;
      }
    bar();
  }
  STAMP(1);
  score_loop<KIND>(c, h, W, lds);
}

DEV void job_c(const Params& p, Ctx c, int h, int cq, ldsp lds) {
  relane(c);
  const int n0 = cq * 144;
  for (int pc = c.w; pc < 40; pc += 4) {
    int q = pc * 64 + c.lane, row = q / 20, ch = q - row * 20;
    dma16(p.wo + (size_t)(h * 128 + row) * 1152 + n0 * 2 + (ch < 18 ? ch : 0) * 16, lds + pc * 1024);
  }
  const int gr = c.c16 >> 2, rg = c.c16 & 3;
  const int drow = 16 * (gr >> 1) + 4 * (gr & 1) + (rg & 1) + (rg >> 1) * 8;
  auto rows = [&](int u, int at) {
#pragma unroll
    for (int q = 0; q < 8; ++q) dma16(p.wv + (size_t)(u * 32 + q * 4 + c.g) * 8192 + h * 256 + ((c.c16 ^ (c.g & 1) ^ (q << 1)) << 4), lds + at + q * 1024);
  };
  auto landed = [&] { if (c.w < 2) asm volatile("s_waitcnt vmcnt(32)" ::: "memory"); else asm volatile("s_waitcnt vmcnt(24)" ::: "memory"); };
  for (int k = 0; k < 3; ++k) rows(c.w + 4 * k, 40960 + (c.w * 3 + k) * 8192);
  if (c.w < 2) rows(16 + c.w, 139264 + c.w * 8192);
  landed(); bar();
  u32x4 B[9][4];
  {
    const int q = (c.lane >> 2) & 3, pp = c.lane & 3;
#pragma unroll
    for (int i = 0; i < 9; ++i)
#pragma unroll
      for (int ks = 0; ks < 4; ++ks) {
        ldsp a = lds + (ks * 32 + c.g * 8 + q) * 320 + i * 32 + pp * 8;
        u32x2 x = trd(a), y = trd(a + 4 * 320);
        B[i][ks] = u32x4{x.x, x.y, y.x, y.y};
        asm volatile("" : "+a"(B[i][ks]));
      }
  }
  const int dst = C_O + (n0 / 8 + (c.c16 >> 3)) * 294912 + (h >> 3) * 73728 + ((h & 7) >> 1) * 2048 + (h & 1) * 512 + c.g * 128 + (c.c16 & 7) * 16;
  bar();
  STAMP(12);
  rows(c.w + 12, c.w * 8192);
  const int fo = c.lane == 0 ? FL + (F_C + cq * 32 + h) * 128 + c.w * 4 : OOB;
  const int fs = (rg & 1) | ((gr & 1) << 1) | ((rg >> 1) << 2) | ((gr >> 1) << 3);
  auto img = [&](int k) { return lds + (k == 3 ? 139264 + c.w * 8192 : k >= 4 ? c.w * 8192 : 40960 + (c.w * 3 + k) * 8192) + drow * 256; };
  u32x4 A[2][4];
  auto frag = [&](ldsp im, int ks) { ldsp a = im + (((ks * 4 + c.g) ^ fs) << 4); A[0][ks] = *(LAS u32x4*)a; A[1][ks] = *(LAS u32x4*)(a + 512); };
  landed();
#pragma unroll
  for (int ks = 0; ks < 4; ++ks) frag(img(0), ks);
  f32x4 d[3][2] = {};
  int d8 = OOB, so = 0;
  asm volatile("" : "+v"(d8));
#pragma clang loop unroll(disable)
  for (int k = 0, kn; k < 5; k = kn) {
    kn = k == 2 && c.w >= 2 ? 4 : k + 1;
    const int u = k == 3 ? 16 + c.w : c.w + 4 * min(k, 3), sp = so;
    so = (u >> 1) * 8192 + (u & 1) * 1024;
    ldsp nx = img(kn);
#pragma unroll
    for (int i = 0; i < 9; ++i) {
      f32x4 (&e)[2] = d[(i + 2) % 3];
      f32x4 (&f)[2] = d[i % 3];
      if (i == CPUB && k) { asm volatile("s_waitcnt vmcnt(%0)" :: "i"(CPUB - 1) : "memory"); st4((unsigned)(k - 1), c.ws, fo, 0); }
      if (i == 8) landed();
      u32x4 o;
#pragma unroll
      for (int q = 0; q < 8; ++q) {
        const int ks = q >> 1, y = q & 1, z = q - 3;
        if (ks == 0) asm volatile("v_mfma_f32_16x16x32_bf16 %0, %1, %2, 0" : "=&v"(f[y]) : "v"(A[y][ks]), "a"(B[i][ks]));
        else asm volatile("v_mfma_f32_16x16x32_bf16 %0, %1, %2, %0" : "+v"(f[y]) : "v"(A[y][ks]), "a"(B[i][ks]));
        if (q == 7) st16(o, c.ws, i ? dst + (i - 1) * 2 * 294912 : d8, i ? so : sp);
        else if (q >= 3) asm volatile("v_cvt_pk_bf16_f32 %0, %1, %2" : "=v"(o[z & 3]) : "v"(e[z >> 1 & 1][(z & 1) * 2]), "v"(e[z >> 1 & 1][(z & 1) * 2 + 1]));
        if (i == 8 && y) frag(nx, ks);
        SB();
      }
    }
    d8 = dst + 16 * 294912;
  }
  asm volatile("s_nop 7" : "+v"(d[2][0]), "+v"(d[2][1]));
  st16(pk8(d[2][0], d[2][1]), c.ws, d8, so);
  vm0();
  st4(4u, c.ws, fo, 0);
  bar();
  STAMP(1);
}

DEV void mixer_seed(const Params& p, Ctx c, int j) {
  relane(c);
  unsigned base;
  const int po = get_pol(c, base), q = (c.c16 >> 3) * 8 + (c.g >> 1) * 4;
  unsigned v = *(const unsigned*)(p.mem0 + (c.c16 & 7) * 1152 + j * 16 + q);
  if (!(c.g & 1)) st4(v, c.ws, mapo(MA_O + j * 128 + (c.c16 & 7) * 16 + q) + po, (RING - 1) * SLOT_PH);
}
template <bool TIM> DEV void role_mixer(const Params& p, Ctx c, int j, ldsp lds) {
  relane(c);
  Clk ck = {0, 0, cyc(), 0.f, 0, 0, 480, 330, false};
  const unsigned pv = ld4(c.ws, FL + F_POL * 128, 0);
  u32x8 W[9][4];
  int stage = -1;
#pragma unroll
  for (int q = 0; q < 9; ++q) {
    const int t = q < 6 ? q : q == 6 ? 8 : q - 1, v = q < 7 ? q >> 1 : 4;
    while (stage < v) {
      int x = (int)ld4(c.ws, FL + (F_C + (j / 18) * 32 + c.w * 8 + ((c.lane >> 2) & 7)) * 128 + (c.lane & 3) * 4, 0);
      if (__all(x >= v)) { stage = __all(x >= 4) ? 4 : __all(x >= v + 1) ? v + 1 : v; break; }
      if (expired(c)) break;
      __builtin_amdgcn_s_sleep(4);
    }
    asm volatile("" ::: "memory");
    if (q == 0) STAMP(2);
#pragma unroll
    for (int i = 0; i < 4; ++i) {
      int o = C_O + j * 294912 + c.w * 73728 + (c.c16 >> 3) * 512 + c.g * 128 + (c.c16 & 7) * 16;
      u32x4 a = ld16(c.ws, o, t * 8192 + i * 2048), b = ld16(c.ws, o, t * 8192 + i * 2048 + 1024);
      W[t][i] = u32x8{a.x, a.y, a.z, a.w, b.x, b.y, b.z, b.w};
    }
  }
  int known = -1, fx = peek_flags(c, F_U + c.w * 8, 8, 0, known);
  ck.base = (unsigned)__builtin_amdgcn_readfirstlane((int)pv);
  if ((int)ck.base < 0) get_pol(c, ck.base);
  const int po = (ck.base & 1) * 8192;
  PIN_W(W);
  STAMP(3);
  relane(c);
  const int r8 = c.c16 & 7, blk = c.c16 >> 3;
  const int mb0 = mapo(MA_O + c.lane * 16) + po, mb8 = mapo(MA_O + 4096 + c.lane * 16) - 4096 + po;
  int pxo[4];
#pragma unroll
  for (int i = 0; i < 4; ++i) { int o = 2 * c.g + blk; pxo[i] = mapo(P_O + (c.w * 8 + i * 2 + (o >> 2)) * 640 + 128 + (o & 3) * 128 + r8 * 16) + po; }
  const int pmo = mapo(P_O + (c.w * 8 + 2 * (c.g >> 1) + blk + (c.g & 1) * 4) * 640 + r8 * 16) + po;
  const int mo = c.lane < 16 ? mapo(MA_O + j * 128 + r8 * 16 + blk * 8) + po : OOB, so3 = c.lane == 0 ? ST_O + po + 1024 + (j >> 2) * 128 + (j & 3) * 4 : OOB;
  const int zo = Z_O + j * 20480 + c.w * 5120 + c.lane * 16;
  const int sp = TIM && c.lane < 16 ? ST_O + po + r8 * 128 : OOB, sm = TIM && c.lane < 18 ? ST_O + po + 1024 + c.lane * 128 : OOB;
  const int fy = c.w == 1 && c.lane == 0 ? FL + (F_Y + j) * 128 : OOB;
  u32x4 zy = {0, 0, 0, 0};
  ldsp ub = lds + 16384 + c.w * 4096;
  auto bulk = [&](int ny, int nu) {
    st16(zy, c.ws, ny >= 0 ? zo : OOB, ny * 1474560 + 4096);
#pragma unroll
    for (int i = 0; i < 4; ++i) __builtin_amdgcn_raw_ptr_buffer_load_lds(c.ws, ub + i * 1024, 16, zo, nu * 1474560 + i * 1024, 0, SC1);
  };
  wait_flags_k(c, F_U + c.w * 8, 8, 0, known, fx);
  bulk(-1, 0);
  ck.rm = G_MMIN;
  for (int n = 0; n < 64; ++n) {
    PIN_W(W);
    u32x4 zu[4];
    f32x4 acc[4];
    if (ZPOS >= 0) wait_flags_k(c, F_U + c.w * 8, 8, n + 1, known, fx);
    if (TIM) wait_until(ck, ck.rm + D_MX);
    bar();
    u32x4 stv;
    int dbgp = 0, tm = 0;
    project_l(c, mb0, mb8, ((n - 1) & (RING - 1)) * SLOT_PH, W, acc, lds + 4096, [&](int t, bool first) {
      if (t == 0) {
        if (TIM) stv = ld16(c.ws, sm, mapo(((n - 1) & 63) * 4096));
        if (first) {
#pragma unroll
          for (int i = 0; i < 4; ++i) zu[i] = *(LAS u32x4*)(ub + i * 1024 + c.lane * 16);
        }
      }
      if (t == 1) fx = peek_flags(c, F_U + c.w * 8, 8, ZPOS >= 0 ? n + 2 : n + 1, known);
      if (t == 1 && ZPOS < 0) st4((unsigned)(n - 1), c.ws, fy, 0);
      if (t == ZPOS && first) { asm volatile("" : "+v"(zu[0]), "+v"(zu[1]), "+v"(zu[2]), "+v"(zu[3])); bulk(n - 1, min(n + 1, 63)); }
      if (t == 9 && TIM) tm = latest<true>(stv);
      if (t >= 1000) dbgp = t - 1000;
    });
    {
      float a[4], b[4];
#pragma unroll
      for (int r = 0; r < 4; ++r) { a[r] = fold32(acc[0][r], acc[1][r]); b[r] = fold32(acc[2][r], acc[3][r]); }
      auto y0 = __builtin_amdgcn_permlane16_swap(pk(a[0], a[1]), pk(b[0], b[1]), false, false), y1 = __builtin_amdgcn_permlane16_swap(pk(a[2], a[3]), pk(b[2], b[3]), false, false);
      zy = u32x4{y0[0], y1[0], y0[1], y1[1]};
    }
    if (__builtin_expect(n == 63, 0)) break;
    int tb = 0, need = 0; unsigned mb = 0;
    asm volatile("" : "+v"(zy), "+v"(zu[0]), "+v"(zu[1]), "+v"(zu[2]), "+v"(zu[3]), "+v"(fx));
    if (ZPOS < 0) wait_flags_k(c, F_U + c.w * 8, 8, n + 1, known, fx);
    if (TIM) {
      if (__builtin_expect((unsigned)tm <= (unsigned)(ck.rm - G_MMIN), 1)) { need = ck.ok ? tm + G_M - ck.rp : ck.dm; ck.ok = true; ck.rp = ck.rm + ck.ds; }
      else { plan_p(c, ck, n, tm, sm, sp); need = ck.dm; }
      tb = wait_until(ck, ck.rp - T_CAL); mb = cyc();
    }
    bar();
    const int so = (n & (RING - 1)) * SLOT_PH;
    f32x4 d;
    int dbgcnt = 0;
    for (;;) {
      u32x4 px[4], pm;
#pragma unroll
      for (int i = 0; i < 4; ++i) px[i] = ld16(c.ws, pxo[i], so);
      pm = ld16(c.ws, pmo, so);
      if (TIM) stv = ld16(c.ws, sp, mapo(n * 4096));
      SB();
      if (ZPOS == -1) { if (ZSL) __builtin_amdgcn_s_sleep(ZSL); bulk(n, n + 1); }
      if (TIM) { ck.dm = adapted(ck.dm, need); need = ck.dm; clk_set(ck, tb, mb); asm volatile("" : "+s"(ck.dm), "+v"(ck.k)); }
      SB();
      unsigned mx = 0;
      MFMA16_Z(d, zu[0], px[0]);
#pragma unroll
      for (int i = 0; i < 4; ++i) {
        if (i) MFMA16_ACC(d, zu[i], px[i]);
        mx = umax_(umax_(mx, px[i].x), px[i].y); mx = umax_(umax_(mx, px[i].z), px[i].w);
        asm volatile("" : "+v"(mx));
      }
      MFMA16_ACC(d, zy, pm);
      mx = umax_(umax_(mx, pm.x), pm.y); mx = umax_(umax_(mx, pm.z), pm.w);
      if (__builtin_expect(!__any(mx == 0xFFFFFFFFu), 1)) break;
      if (expired(c)) break;
      __builtin_amdgcn_s_sleep(4);
      DBGCNT();
    }
    asm volatile("s_nop 7" : "+v"(d));
    if ((c.g >> 1) == blk) *(LAS f32x4*)(lds + ((c.w * 2 + blk) * 16 + (c.g & 1) * 8 + r8) * 16) = d;
    bar();
    if (!TIM && c.w == 0) {
      f32x4 a = *(LAS f32x4*)(lds + (c.g * 32 + c.c16) * 16), b = *(LAS f32x4*)(lds + (c.g * 32 + 16 + c.c16) * 16);
      a += b;
      float s0 = x16sum(fold32(a[0], a[2])), s1 = x16sum(fold32(a[1], a[3]));
      unsigned u = pk(s0, s1);
      auto t2 = __builtin_amdgcn_permlane32_swap(u, u, false, false);
      st8(u32x2{t2[0], t2[1]}, c.ws, mo, so);
      st8(u32x2{~0u, ~0u}, c.ws, mo, ((n + RING / 2) & (RING - 1)) * SLOT_PH);
    } else if (TIM) {
      unsigned me = cyc(); int ts = clk_at(ck, me), tp = latest<false>(stv);
      __builtin_amdgcn_s_sleep(3); SB();
      st4((unsigned)ts, c.ws, so3, mapo(n * 4096));
      if (ZPOS == -2) bulk(n, n + 1);
      plan_m(c, ck, n, tp, sm, sp);
      if (n < 60) DBGW(17 + n * 4, tb - ck.rp);
    } else if (ZPOS != -1) __builtin_amdgcn_s_sleep(5);
    if (ZPOS == -2 && !TIM) bulk(n, n + 1);
    if (ZPOS >= 0) st4((unsigned)(n - 1), c.ws, fy, 0);
    DBGW(512 + n * 4 + c.w, dbgcnt); DBGW(768 + n * 4 + c.w, dbgp);
  }
  st16(zy, c.ws, zo, 63 * 1474560 + 4096);
  vm0(); bar();
  if (c.w == 1) set_flag(c, F_Y + j, 63);
  DBGEND(0);
}

template <int K0, int N> DEV void take_wt(const Ctx& c, ldsp img, u32x4 (&F)[18][2]) {
  const int q = (c.lane >> 2) & 3, pp = c.lane & 3;
#pragma unroll
  for (int ks = 0; ks < N; ++ks)
#pragma unroll
    for (int i = 0; i < 2; ++i) {
      int row = ks * 32 + c.g * 8 + q;
      u32x2 x = trd(w_addr(img, row, c.w * 32 + i * 16, pp)), y = trd(w_addr(img, row + 4, c.w * 32 + i * 16, pp));
      F[K0 + ks][i] = u32x4{x.x, x.y, y.x, y.y};
    }
}

DEV void role_vu(const Params& p, Ctx c, int h, ldsp lds) {
  relane(c);
  u32x4 WV[18][2], WO[9][4];
  const XSt X = x_init(c, p.bx, lds, 8 + (h & 7));
  for (int s = 0; s < 8; ++s) x_touch(X, s);
  stage_w(c, p.wv, h, 0, 576, lds);
  vm0(); bar();
  take_wt<0, 18>(c, lds, WV);
  bar();
  for (int pc = c.w; pc < 148; pc += 4) {
    int q = pc * 64 + c.lane, row = q / 74, ch = q - row * 74;
    dma16(p.wo + (size_t)(h * 128 + row) * 1152 + (ch < 72 ? ch : 0) * 16, lds + pc * 1024);
  }
  vm0(); bar();
  {
    const int q = (c.lane >> 2) & 3, pp = c.lane & 3;
#pragma unroll
    for (int i = 0; i < 9; ++i)
#pragma unroll
      for (int ks = 0; ks < 4; ++ks) {
        ldsp a = lds + (ks * 32 + c.g * 4 + q) * 1184 + c.w * 288 + i * 32 + pp * 8;
        u32x2 x = trd(a), y = trd(a + 16 * 1184);
        WO[i][ks] = u32x4{x.x, x.y, y.x, y.y};
      }
  }
  bar();
#pragma unroll
  for (int i = 0; i < 9; ++i) x_piece(X, 0, 0, i);
#pragma unroll
  for (int i = 0; i < 9; ++i) x_piece(X, 1, 36864, i);
  const int dst = Z_O + (c.w * 18 + (c.c16 >> 3)) * 20480 + (h >> 3) * 5120 + ((h & 7) >> 1) * 1024 + ((h & 1) * 4 + c.g) * 128 + (c.c16 & 7) * 16;
  const int fo = c.w == 0 && c.lane == 0 ? FL + (F_U + h) * 128 : OOB;
  unsigned vx = (unsigned)(size_t)lds + 114688 + c.lane * 16, xa[2] = {X.xe, X.xe ^ 64};
  int bo = 0;
  u32x4 b[4][2], a[4][2];
  f32x4 e0 = {0, 0, 0, 0}, e1 = e0;
  vm0(); bar();
  X_READ(b[0], xa, 0); X_READ(b[1], xa, 1); X_READ(b[2], xa, 2);
  for (int s = 0; s < 65; ++s) {
#pragma unroll
    for (int k = 0; k < 18; ++k) { asm volatile("" : "+a"(WV[k][0])); asm volatile("" : "+a"(WV[k][1])); }
#pragma unroll
    for (int i = 0; i < 5; ++i)
#pragma unroll
      for (int k = 0; k < 4; ++k) asm volatile("" : "+a"(WO[i][k]));
    const int b1 = x_next(bo), b2 = x_next(b1), d1 = s >= 1 ? dst : OOB;
    f32x4 v[2][2] = {};
#pragma unroll
    for (int ks = 0; ks < 18; ++ks) {
      if (ks < 15) { X_READ(b[(ks + 3) & 3], xa, ks + 3); LGKM(6, b[ks & 3]); }
      else { asm volatile("ds_read_b128 %0, %2 offset:%3\n\tds_read_b128 %1, %2 offset:%4" : "=&v"(a[ks - 15][0]), "=&v"(a[ks - 15][1]) : "v"(vx ^ 8192), "i"((ks - 15) * 1024), "i"((ks - 15) * 1024 + 4096) : "memory"); LGKM(6, b[ks & 3]); }
      SB();
#pragma unroll
      for (int i = 0; i < 2; ++i) { v[0][i] = mfma16(WV[ks][i], b[ks & 3][0], v[0][i]); v[1][i] = mfma16(WV[ks][i], b[ks & 3][1], v[1][i]); }
      if (ks == 1) st16(pk8(e0, e1), c.ws, s >= 2 ? dst : OOB, (s - 2) * 1474560 + 8 * 40960);
      if (ks == 2) {
        if (s == 2) vm0();
        asm volatile("s_waitcnt vmcnt(10)\n\ts_barrier" ::: "memory"); st4((unsigned)(s == 2 ? 0 : s - 3), c.ws, fo, 0); STAMP(16 + s * 4);
      }
      if (ks >= 3 && ks < 12) x_piece(X, s + 2, b2, ks - 3);
      if (ks == 12) x_touch(X, s + 8);
      SB();
    }
    asm volatile("ds_read_b128 %0, %2 offset:3072\n\tds_read_b128 %1, %2 offset:7168" : "=&v"(a[3][0]), "=&v"(a[3][1]) : "v"(vx ^ 8192) : "memory");
    xa[0] = X.xe + b1; xa[1] = (X.xe ^ 64) + b1; bo = b1;
    LGKM(2, a[0]); LGKM(2, a[1]); LGKM(2, a[2]);
#pragma unroll
    for (int i = 0; i < 9; ++i) {
      f32x4 d0 = {0, 0, 0, 0}, d1_ = d0;
#pragma unroll
      for (int ks = 0; ks < 4; ++ks) {
        if (i == 0 && ks == 3) { SB(); LGKM(0, a[3]); }
        d0 = mfma16(a[ks][0], WO[i][ks], d0); d1_ = mfma16(a[ks][1], WO[i][ks], d1_);
      }
      if (i > 0) st16(pk8(e0, e1), c.ws, d1, (s - 1) * 1474560 + (i - 1) * 40960);
      if (i == 1) { u32x4 t = pk8(v[0][0], v[0][1]); LDSW("ds_write_b128", vx + c.w * 1024, t, 0); }
      if (i == 2) { u32x4 t = pk8(v[1][0], v[1][1]); LDSW("ds_write_b128", vx + c.w * 1024, t, 4096); }
      if (i == 8) { X_READ(b[0], xa, 0); X_READ(b[1], xa, 1); X_READ(b[2], xa, 2); }
      e0 = d0; e1 = d1_;
      SB();
    }
    vx ^= 8192;
  }
  LGKM(0, b[0]); LGKM(0, b[1]); LGKM(0, b[2]);
  SB();
  st16(pk8(e0, e1), c.ws, dst, 63 * 1474560 + 8 * 40960);
  vm0(); bar();
  st4(1000u, c.ws, fo, 0);
}

DEV void role_qk(const Params& p, Ctx c, int h, ldsp lds) {
  relane(c);
  u32x4 WQ[18][2], WK[18][2];
  const XSt X = x_init(c, p.bx, lds, h & 7);
  for (int s = 0; s < 8; ++s) x_touch(X, s);
  stage_w(c, p.wq, h, 288, 288, lds, 24); stage_w(c, p.wk, h, 288, 288, lds + 73728, 24);
  vm0(); STAMP(13); bar();
  take_wt<9, 9>(c, lds, WQ); take_wt<9, 9>(c, lds + 73728, WK);
  bar(); STAMP(14);
  stage_w(c, p.wq, h, 0, 288, lds); stage_w(c, p.wk, h, 0, 288, lds + 73728);
  vm0(); STAMP(15); bar();
  take_wt<0, 9>(c, lds, WQ); take_wt<0, 9>(c, lds + 73728, WK);
  bar();
  STAMP(12);
#pragma unroll
  for (int i = 0; i < 9; ++i) x_piece(X, 0, 0, i);
#pragma unroll
  for (int i = 0; i < 9; ++i) x_piece(X, 1, 36864, i);
  const int dst = KX_O + h * 16384 + c.w * 1024 + c.lane * 16;
  const int fo = c.w == 0 && c.lane == 0 ? FL + (F_KX + h) * 128 : OOB;
  unsigned xa[2] = {X.xe, X.xe ^ 64};
  int bo = 0;
  u32x4 b[4][2];
  f32x4 r0[4][2] = {}, r1[4][2];
  vm0(); bar();
  X_READ(b[0], xa, 0); X_READ(b[1], xa, 1);
  auto seg = [&](int s, f32x4 (&r)[4][2], f32x4 (&e)[4][2], int ph) {
#pragma unroll
    for (int k = 0; k < 18; ++k) { asm volatile("" : "+a"(WQ[k][0])); asm volatile("" : "+a"(WQ[k][1])); }
#pragma unroll
    for (int k = 0; k < 6; ++k) { asm volatile("" : "+a"(WK[k][0])); asm volatile("" : "+a"(WK[k][1])); }
    const int b1 = x_next(bo), b2 = x_next(b1), d1 = s >= 1 ? dst : OOB;
#pragma unroll
    for (int ks = 0; ks < 18; ++ks) {
      if (ks == 16) { xa[0] = X.xe + b1; xa[1] = (X.xe ^ 64) + b1; bo = b1; }
      X_READ(b[(ks + 2 + ph) & 3], xa, (ks + 2) % 18); LGKM(4, b[(ks + ph) & 3]);
      SB();
#pragma unroll
      for (int i = 0; i < 2; ++i)
#pragma unroll
        for (int tau = 0; tau < 2; ++tau) {
          r[tau][i] = mfma16(WK[ks][i], b[(ks + ph) & 3][tau], ks ? r[tau][i] : f32x4{0, 0, 0, 0});
          r[2 + tau][i] = mfma16(WQ[ks][i], b[(ks + ph) & 3][tau], ks ? r[2 + tau][i] : f32x4{0, 0, 0, 0});
        }
      if (ks < 4) st16(pk8(e[ks][0], e[ks][1]), c.ws, d1, (s - 1) * 524288 + ks * 4096);
      if (ks == 4) {
        if (s == 1) vm0();
        asm volatile("s_waitcnt vmcnt(5)\n\ts_barrier" ::: "memory"); st4((unsigned)(s == 1 ? 0 : s - 2), c.ws, fo, 0); STAMP(16 + s * 4);
      }
      if (ks >= 5 && ks < 14) x_piece(X, s + 2, b2, ks - 5);
      if (ks == 14) x_touch(X, s + 8);
      SB();
    }
  };
  for (int s = 0; s < 64; s += 2) { seg(s, r1, r0, 0); seg(s + 1, r0, r1, 2); }
  LGKM(0, b[0]); LGKM(0, b[1]);
  SB();
#pragma unroll
  for (int k = 0; k < 4; ++k) st16(pk8(r0[k][0], r0[k][1]), c.ws, dst, 63 * 524288 + k * 4096);
  vm0(); bar();
  st4(1000u, c.ws, fo, 0);
}

template <int NI> DEV void job_out(const Params& p, Ctx c, int s, int x, ldsp lds) {
  relane(c);
  u32x4 B[10][2], A[8][NI], Y[2][NI];
  const int pxs = s * 81920, zs = s * 1474560;
  const int zb = Z_O + (x * 2 * NI + (c.c16 >> 3)) * 20480 + c.w * 5120 + (c.c16 & 7) * 16;
  wait_flags(c, F_U + c.w * 8, 8, s);
#pragma unroll
  for (int i = 0; i < NI; ++i)
#pragma unroll
    for (int k = 0; k < 8; ++k) A[k][i] = ld16(c.ws, zb + i * 40960 + k * 512 + c.g * 128, zs);
#pragma unroll
  for (int i = 0; i < NI - 2; ++i)
#pragma unroll
    for (int k = 0; k < 8; ++k) asm volatile("" : "+a"(A[k][i]));
  wait_flags(c, F_Y + x * 2 * NI, 2 * NI, s, F_PX + c.w * 16, 16);
  STAMP(16 + s * 4);
#pragma unroll
  for (int k = 0; k < 8; ++k)
#pragma unroll
    for (int tq = 0; tq < 2; ++tq) B[k][tq] = ld16(c.ws, PX_O + (c.w * 8 + k) * 2560 + tq * 1280 + c.lane * 16, pxs);
#pragma unroll
  for (int k = 0; k < 2; ++k)
#pragma unroll
    for (int tq = 0; tq < 2; ++tq) B[8 + k][tq] = ld16(c.ws, PX_O + (c.w * 8 + 2 * (c.g >> 1) + k + (c.g & 1) * 4) * 2560 + tq * 1280 + 1024 + c.c16 * 16, pxs);
#pragma unroll
  for (int i = 0; i < NI; ++i)
#pragma unroll
    for (int k = 0; k < 2; ++k) Y[k][i] = ld16(c.ws, zb + i * 40960 + 4096 + c.g * 256 + k * 128, zs);
  SB();
#pragma unroll
  for (int i = 0; i < NI; ++i) {
    f32x4 d0 = {0, 0, 0, 0}, d1 = d0;
#pragma unroll
    for (int k = 0; k < 10; ++k) { u32x4 a = k < 8 ? A[k & 7][i] : Y[k & 1][i]; d0 = mfma16(a, B[k][0], d0); d1 = mfma16(a, B[k][1], d1); }
    *(LAS f32x4*)(lds + ((c.w * 18 + i * 2) * 64 + c.lane) * 16) = d0;
    *(LAS f32x4*)(lds + ((c.w * 18 + i * 2 + 1) * 64 + c.lane) * 16) = d1;
  }
  bar();
#pragma unroll
  for (int u = 0; u < (NI + 1) / 2; ++u) {
    int t = c.w + 4 * u, i = t >> 1, tq = t & 1;
    if (t < 2 * NI) {
      f32x4 d = *(LAS f32x4*)(lds + (t * 64 + c.lane) * 16);
#pragma unroll
      for (int ww = 1; ww < 4; ++ww) d += *(LAS f32x4*)(lds + ((ww * 18 + t) * 64 + c.lane) * 16);
      int tok = 8 * (c.c16 >> 2) + 4 * tq + (c.c16 & 3);
      *(u32x2*)(p.out + (size_t)(s * 32 + tok) * 2304 + 1152 + (x * 16 * NI + i * 16 + c.g * 4) * 2) = u32x2{pk(d[0], d[1]), pk(d[2], d[3])};
    }
  }
  bar();
  STAMP(17 + s * 4);
}

template <int RS> DEV void a_wfrags(const Ctx& c, ldsp lds, u32x4 (&W)[18][4], int o0, int o1, int o2, int o3) {
  ldsp a0 = lds + (c.g * 16 + ((c.lane >> 2) & 3)) * RS;
#pragma unroll
  for (int ks = 0; ks < 18; ++ks)
#pragma unroll
    for (int i = 0; i < 4; ++i) {
      ldsp a = a0 + (i == 0 ? o0 : i == 1 ? o1 : i == 2 ? o2 : o3) + ((ks >> 1) * 64 + (ks & 1) * 8) * RS;
      u32x2 x = trd(a), y = trd(a + 4 * RS);
      W[ks][i] = u32x4{x.x, x.y, y.x, y.y};
    }
}
#define PIN_A(W) do { _Pragma("unroll") for (int k_ = 0; k_ < 15; ++k_) _Pragma("unroll") for (int i_ = 0; i_ < 4; ++i_) asm volatile("" : "+a"(W[k_][i_])); } while (0)
struct Ring { rsrc_t r; int v0, v1; ldsp base; unsigned ra; u32x4 x0, x1; };
template <int AUX> DEV void ring_fill(const Ring& R, int so, int i) {
  __builtin_amdgcn_raw_ptr_buffer_load_lds(R.r, R.base + i * 2048, 16, R.v0, so + i * 128, 0, AUX);
  __builtin_amdgcn_raw_ptr_buffer_load_lds(R.r, R.base + i * 2048 + 1024, 16, R.v1, so + i * 128, 0, AUX);
}
#define RING_READ(y0, y1, R, i) asm volatile("s_waitcnt vmcnt(16)\n\tds_read_b128 %0, %2 offset:%4\n\tds_read_b128 %1, %3 offset:%4" : "=&v"(y0), "=&v"(y1) : "v"(R.ra), "v"(R.ra ^ 16), "i"((i) * 2048) : "memory")
template <int AUX> DEV void ring_init(const Ctx& c, Ring& R, rsrc_t r, ldsp base, int so) {
  R.r = r; R.base = base;
  R.v0 = (c.lane >> 3) * 1152 + (((c.lane & 7) ^ c.g) << 4); R.v1 = (R.v0 ^ 64) + 9216;
  R.ra = (unsigned)(size_t)base + (c.c16 >> 3) * 1024 + (c.c16 & 7) * 128 + (((c.g * 2) ^ (c.c16 >> 1)) << 4);
#pragma unroll
  for (int i = 0; i < 9; ++i) ring_fill<AUX>(R, so, i);
}
template <int AUX> DEV void ring_first(Ring& R, int s1) {
  RING_READ(R.x0, R.x1, R, 0);
  asm volatile("s_waitcnt lgkmcnt(0)" : "+v"(R.x0), "+v"(R.x1) :: "memory");
  ring_fill<AUX>(R, s1, 0);
}
template <int AUX, class F> DEV void ring_tile(Ring& R, int s1, int s2, F mm) {
#pragma unroll
  for (int i = 0; i < 9; ++i) {
    u32x4 y0, y1;
    RING_READ(y0, y1, R, (i + 1) % 9);
    SB();
    mm(2 * i, R.x0); mm(2 * i + 1, R.x1);
    SB();
    asm volatile("s_waitcnt lgkmcnt(0)" : "+v"(y0), "+v"(y1) :: "memory");
    ring_fill<AUX>(R, i == 8 ? s2 : s1, (i + 1) % 9);
    R.x0 = y0; R.x1 = y1;
    SB();
  }
}
constexpr float SCL_A = 0.23570226039551584f * 1.4426950408889634f;
constexpr int AREC = 2368, AHALF = 32 * AREC;
DEV float max16(f32x16 s, float m) {
#pragma unroll
  for (int i = 0; i < 16; i += 2) m = __builtin_fmaxf(__builtin_fmaxf(m, s[i]), s[i + 1]);
  return m;
}
DEV float x32max(float x) { auto t = __builtin_amdgcn_permlane32_swap(f2u(x), f2u(x), false, false); return fmaxf(u2f(t[0]), u2f(t[1])); }
DEV void a_pv(f32x16 s, u32x4 v0, u32x4 v1, f32x16& o) {
  float e[16];
#pragma unroll
  for (int i = 0; i < 16; ++i) e[i] = __builtin_amdgcn_exp2f(s[i]);
  o = mfma32(v0, u32x4{pk(e[0], e[1]), pk(e[2], e[3]), pk(e[4], e[5]), pk(e[6], e[7])}, o);
  o = mfma32(v1, u32x4{pk(e[8], e[9]), pk(e[10], e[11]), pk(e[12], e[13]), pk(e[14], e[15])}, o);
}
#define EX4(s, i) do { s[i] = __builtin_amdgcn_exp2f(s[i]); s[i + 1] = __builtin_amdgcn_exp2f(s[i + 1]); s[i + 2] = __builtin_amdgcn_exp2f(s[i + 2]); s[i + 3] = __builtin_amdgcn_exp2f(s[i + 3]); } while (0)
#define MX8(m, t, i) do { m = fmaxf(fmaxf(m, t[i]), t[i + 1]); m = fmaxf(fmaxf(m, t[i + 2]), t[i + 3]); m = fmaxf(fmaxf(m, t[i + 4]), t[i + 5]); m = fmaxf(fmaxf(m, t[i + 6]), t[i + 7]); } while (0)
struct AState { u32x4 qa0, qa1, qb0, qb1, k0, k1; f32x16 oa, ob, na, nb; ldsp kp, k1p, vp; };
template <int O> DEV void a_step(AState& z, f32x16& sa, f32x16& sb, f32x16& ta, f32x16& tb) {
  u32x4 pa, pb, pc, pd;
  asm volatile("" : "+v"(z.k0));
  MFMA32_C(ta, z.k1, z.qa1, z.na); EX4(sa, 0); SB();
  u32x4 v0 = *(LAS u32x4*)(z.vp + O), v1 = *(LAS u32x4*)(z.vp + O + 608);
  MFMA32_ACC(ta, z.k0, z.qa0); EX4(sa, 4); pa.x = pk(sa[0], sa[1]); pa.y = pk(sa[2], sa[3]); SB();
  MFMA32_C(tb, z.k1, z.qb1, z.nb); EX4(sa, 8); pa.z = pk(sa[4], sa[5]); pa.w = pk(sa[6], sa[7]); SB();
  MFMA32_ACC(tb, z.k0, z.qb0); EX4(sa, 12); pb.x = pk(sa[8], sa[9]); pb.y = pk(sa[10], sa[11]); SB();
  z.k0 = *(LAS u32x4*)(z.kp + O + 2 * AREC); z.k1.x = *(LAS unsigned*)(z.k1p + O + 2 * AREC);
  float ma = 0.f, mb = 0.f;
  z.oa = mfma32(v0, pa, z.oa); EX4(sb, 0); pb.z = pk(sa[12], sa[13]); pb.w = pk(sa[14], sa[15]); MX8(ma, ta, 0); SB();
  z.oa = mfma32(v1, pb, z.oa); EX4(sb, 4); pc.x = pk(sb[0], sb[1]); pc.y = pk(sb[2], sb[3]); MX8(ma, ta, 8); SB();
  pc.z = pk(sb[4], sb[5]); pc.w = pk(sb[6], sb[7]); EX4(sb, 8); SB();
  z.ob = mfma32(v0, pc, z.ob); EX4(sb, 12); pd.x = pk(sb[8], sb[9]); pd.y = pk(sb[10], sb[11]); pd.z = pk(sb[12], sb[13]); pd.w = pk(sb[14], sb[15]); MX8(mb, tb, 0); SB();
  z.ob = mfma32(v1, pd, z.ob); MX8(mb, tb, 8);
  if (__any(fmaxf(ma, mb) > 10.f)) {
    ma = x32max(ma); mb = x32max(mb);
    float fa = __builtin_amdgcn_exp2f(-ma), fb = __builtin_amdgcn_exp2f(-mb);
#pragma unroll
    for (int i = 0; i < 16; ++i) { z.na[i] -= ma; z.nb[i] -= mb; ta[i] -= ma; tb[i] -= mb; z.oa[i] *= fa; z.ob[i] *= fb; }
    asm volatile("s_nop 1" : "+v"(z.na), "+v"(z.nb));
  }
  SB();
}
DEV void role_a(const Params& p, Ctx c, int h, int half, int pf, int npf, ldsp lds) {
  relane(c);
  const int job = h * 2 + half;
  u32x4 Q0[8]; unsigned Q1[8];
  ldsp own = lds + half * AHALF, oth = lds + (half ^ 1) * AHALF;
  {
    u32x4 W[18][4];
    {
      for (int i = (pf * 4 + c.w) * 64; i < 9216; i += npf * 256)
        __builtin_amdgcn_global_load_lds((const unsigned*)(p.ax + half * 1179648 + (i + c.lane) * 128), (LAS unsigned*)(lds + (half ? 159744 : 83968)), 4, 0, 0);
      for (int pc = c.w; pc < 81; pc += 4) {
        int q = pc * 64 + c.lane, row = q / 9, ch = q - row * 9, rg = ch / 3, k = ch - rg * 3;
        dma16(p.awqkv + row * 3456 + rg * 1152 + h * 36 + (k == 0 ? 0 : k == 2 ? 20 : rg == 2 ? 4 : 16), own + pc * 1024);
      }
    }
    const int s0 = (half * 1024 + c.w * 256) * 1152;
    Ring R;
    ring_init<0>(c, R, __builtin_amdgcn_make_buffer_rsrc((void*)p.ax, 0, 2048 * 1152, 0x00020000), lds + (half ? 0 : 90112) + c.w * 18432, s0);
    asm volatile("s_waitcnt vmcnt(18)" ::: "memory");
    bar();
    STAMP(10);
    { const int pp = c.lane & 3; a_wfrags<144>(c, own, W, 48 + pp * 8, pp * 8, 96 + (pp & 1) * 32 + (pp >> 1) * 8, pp == 0 ? 88 : pp == 1 ? 40 : 120); }
    bar();
    STAMP(11);
    unsigned rec = (unsigned)(size_t)own + c.w * 8 * AREC;
    { u32x4 one = {0x3F803F80u, 0x3F803F80u, 0x3F803F80u, 0x3F803F80u}; LDSW("ds_write_b128", rec + ((c.lane >> 2) & 7) * AREC + (c.lane & 3) * 304, one, 1440); }
    unsigned ka = rec + c.lane * 16, va = rec + (c.g & 1) * 304 + c.c16 * 16 + (c.g >> 1) * 8;
    unsigned ea = rec + (c.g < 2 ? 1024 + c.lane * 4 : 1408 + (c.g & 1) * 608 + ((c.c16 >> 2) & 1) * 304 + ((c.c16 >> 3) * 4 + (c.c16 & 3)) * 2), eb = ea + (c.g < 2 ? 2 : 16);
    const float es = c.g == 1 ? SCL_A : 1.f;
    ring_first<0>(R, s0 + 18432);
    for (int t = 0; t < 16; t += 2) {
      PIN_A(W);
      unsigned kk[2][2], qq[2][2], ee[2];
#pragma unroll
      for (int u = 0; u < 2; ++u) {
        f32x4 k = {0, 0, 0, 0}, q = k, v = k, e = k;
        ring_tile<0>(R, s0 + min(t + u + 1, 15) * 18432, s0 + min(t + u + 2, 15) * 18432, [&](int ks, u32x4 x) {
          k = mfma16(W[ks][0], x, k); q = mfma16(W[ks][1], x, q); v = mfma16(x, W[ks][2], v); e = mfma16(W[ks][3], x, e); });
        kk[u][0] = pk(k[0], k[1]); kk[u][1] = pk(k[2], k[3]);
        qq[u][0] = pk(q[0] * SCL_A, q[1] * SCL_A); qq[u][1] = pk(q[2] * SCL_A, q[3] * SCL_A);
        ee[u] = pk(e[2] * es, e[3] * es);
        u32x2 vv = {pk(v[0], v[1]), pk(v[2], v[3])};
        LDSW("ds_write_b64", va, vv, 1152 + u * 608);
      }
      auto k0 = __builtin_amdgcn_permlane16_swap(kk[0][0], kk[1][0], false, false), k1 = __builtin_amdgcn_permlane16_swap(kk[0][1], kk[1][1], false, false);
      auto q0 = __builtin_amdgcn_permlane16_swap(qq[0][0], qq[1][0], false, false), q1 = __builtin_amdgcn_permlane16_swap(qq[0][1], qq[1][1], false, false);
      auto e0 = __builtin_amdgcn_permlane16_swap(ee[0], ee[1], false, false);
      u32x4 kf = {k0[0], k1[0], k0[1], k1[1]};
      LDSW("ds_write_b128", ka, kf, 0);
      LDSW("ds_write_b16", ea, e0[0], 0); LDSW("ds_write_b16_d16_hi", eb, e0[0], 0);
#pragma unroll
      for (int i = 0; i < 7; ++i) { Q0[i] = Q0[i + 1]; Q1[i] = Q1[i + 1]; }
      Q0[7] = u32x4{q0[0], q1[0], q0[1], q1[1]}; Q1[7] = c.lane < 32 ? e0[1] : 0u;
      ka += AREC; va += AREC; ea += AREC; eb += AREC;
      STAMP(16 + t);
    }
  }
  STAMP(3);
  vm0(); bar();
  for (int pc = c.w; pc < 74; pc += 4) st16(*(LAS u32x4*)(own + pc * 1024 + c.lane * 16), c.ws, AKV_O + pc * 1024 + c.lane * 16, (h * 2 + half) * AHALF);
  vm0(); bar();
  if (c.w == 0) set_flag(c, F_AKV + h * 2 + half, 0);
  wait_flags(c, F_AKV + h * 2 + (half ^ 1), 1, 0);
  for (int pc = c.w; pc < 74; pc += 4)
    __builtin_amdgcn_global_load_lds((const unsigned*)(p.ws + AKV_O + (h * 2 + (half ^ 1)) * AHALF + pc * 1024 + c.lane * 16), (LAS unsigned*)(oth + pc * 1024), 16, 0, SC1);
  vm0(); bar();
  {
    STAMP(4); STAMPC(8);
    const int r = c.lane & 31, hh = c.lane >> 5;
    AState z;
    z.k1 = u32x4{0, 0, 0, 0}; z.qa1 = z.k1; z.qb1 = z.k1;
    for (int pr = 0; pr < 4; ++pr) {
      z.qa0 = Q0[0]; z.qb0 = Q0[1]; z.qa1.x = Q1[0]; z.qb1.x = Q1[1];
#pragma unroll
      for (int i = 0; i < 6; ++i) { Q0[i] = Q0[i + 2]; Q1[i] = Q1[i + 2]; }
      z.kp = lds + c.lane * 16; z.k1p = lds + 1024 + r * 4;
      z.vp = lds + 1152 + hh * 304 + min(r, 18) * 16;
      z.oa = f32x16{}; z.ob = z.oa;
      f32x16 sa, sb, ta, tb;
      {
        z.k0 = *(LAS u32x4*)z.kp; z.k1.x = *(LAS unsigned*)z.k1p;
        sa = mfma32(z.k0, z.qa0, mfma32(z.k1, z.qa1, z.oa)); sb = mfma32(z.k0, z.qb0, mfma32(z.k1, z.qb1, z.ob));
        float ma = x32max(max16(sa, sa[0])), mb = x32max(max16(sb, sb[0]));
#pragma unroll
        for (int i = 0; i < 16; ++i) { z.na[i] = -ma; z.nb[i] = -mb; sa[i] -= ma; sb[i] -= mb; }
        z.k0 = *(LAS u32x4*)(z.kp + AREC); z.k1.x = *(LAS unsigned*)(z.k1p + AREC);
        asm volatile("s_nop 1" : "+v"(z.na), "+v"(z.nb), "+v"(z.qa0), "+v"(z.qa1), "+v"(z.qb0), "+v"(z.qb1), "+v"(z.k1));
      }
      for (int kt = 0; kt < 31; ++kt) { a_step<0>(z, sa, sb, ta, tb); a_step<AREC>(z, ta, tb, sa, sb); z.kp += 2 * AREC; z.k1p += 2 * AREC; z.vp += 2 * AREC; }
      a_step<0>(z, sa, sb, ta, tb);
      {
        u32x4 v0 = *(LAS u32x4*)(z.vp + AREC), v1 = *(LAS u32x4*)(z.vp + AREC + 608);
        a_pv(ta, v0, v1, z.oa); a_pv(tb, v0, v1, z.ob);
      }
      const f32x16 oa = z.oa, ob = z.ob;
      const int oo = AO_O + (half * 1024 + c.w * 256 + pr * 64 + r) * 1152 + h * 36;
      const float ia = __builtin_amdgcn_rcpf(oa[10]), ib = __builtin_amdgcn_rcpf(ob[10]);
      st16(u32x4{pk(oa[0] * ia, oa[1] * ia), pk(oa[2] * ia, oa[3] * ia), pk(oa[4] * ia, oa[5] * ia), pk(oa[6] * ia, oa[7] * ia)}, c.ws, oo + hh * 20, 0);
      st16(u32x4{pk(ob[0] * ib, ob[1] * ib), pk(ob[2] * ib, ob[3] * ib), pk(ob[4] * ib, ob[5] * ib), pk(ob[6] * ib, ob[7] * ib)}, c.ws, oo + hh * 20, 32 * 1152);
      if (hh == 0) { st4(pk(oa[8] * ia, oa[9] * ia), c.ws, oo + 16, 0); st4(pk(ob[8] * ib, ob[9] * ib), c.ws, oo + 16, 32 * 1152); }
    }
  }
  vm0(); bar();
  if (c.w == 0) set_flag(c, F_AO + h * 2 + half, 0);
  STAMP(5); STAMPC(9);
  if (job >= 63) return;
  relane(c);
  const int cg = job % 9, tg = job / 9, t0 = tg * 18 + (tg < 2 ? tg : 2), t1 = t0 + (tg < 2 ? 19 : 18);
  for (int pc = c.w; pc < 72; pc += 4) dma16(p.awo + (pc * 8 + (c.lane >> 3)) * 1152 + cg * 128 + (c.lane & 7) * 16, lds + pc * 1024);
  vm0(); bar();
  u32x4 W[18][4];
  { const int pp = (c.lane & 3) * 32; a_wfrags<128>(c, lds, W, pp, pp + 8, pp + 16, pp + 24); }
  wait_flags(c, F_AO, 64, 0);
  STAMP(6);
  const int tl = t1 - 1 - ((t1 - 1 - t0 - c.w) & 3);
  Ring R;
  ring_init<SC1>(c, R, c.ws, lds + 73728 + c.w * 18432, AO_O + (t0 + c.w) * 18432);
  ring_first<SC1>(R, AO_O + (t0 + c.w + 4) * 18432);
  for (int t = t0 + c.w; t < t1; t += 4) {
    PIN_A(W);
    f32x4 d[4] = {};
    ring_tile<SC1>(R, AO_O + min(t + 4, tl) * 18432, AO_O + min(t + 8, tl) * 18432, [&](int ks, u32x4 x) {
#pragma unroll
      for (int i = 0; i < 4; ++i) d[i] = mfma16(W[ks][i], x, d[i]); });
    char* o = p.out + (size_t)(t * 16 + c.c16) * 2304 + cg * 128 + c.g * 32;
    *(u32x4*)o = pk8(d[0], d[1]); *(u32x4*)(o + 16) = pk8(d[2], d[3]);
  }
  vm0();
  STAMP(7);
}

__global__ void __launch_bounds__(256, 1) fused_kernel(Params p) {
  extern __shared__ __attribute__((aligned(16))) char lds_raw[];
  ldsp lds = (ldsp)lds_raw;
  Ctx c;
  c.ws = __builtin_amdgcn_make_buffer_rsrc((void*)p.ws, 0, WS_END, 0x00020000);
  c.t0 = rt32();
  c.w = __builtin_amdgcn_readfirstlane(threadIdx.x >> 6); c.lane = c.g = c.c16 = 0;
  STAMP(0);
  const int x = blockIdx.x & 7, r = blockIdx.x >> 3;
  {
    const char* pc; asm volatile("s_getpc_b64 %0" : "=s"(pc));
    const int ln = (r & 7) * 256 + (int)threadIdx.x;
    if (ln < 2 * CODE_LINES) __builtin_amdgcn_global_load_lds((const unsigned*)(pc + ln * 64), (LAS unsigned*)(lds + 163584), 4, 0, 0);
  }
  if (x < 4) {
    if (r < 8) {
      if (c.w == 0) role_score<0>(p, c, x * 8 + r, lds);
      else if (c.w == 1) role_score<1>(p, c, x * 8 + r, lds);
      else role_score<2>(p, c, x * 8 + r, lds);
      return;
    }
  } else {
    if (r < 8) {
      role_qk(p, c, (x - 4) * 8 + r, lds);
      if (r < 6) for (int s = 58 + (r & 1); s < 64; s += 4) job_out<3>(p, c, s, (x & 3) * 3 + (r >> 1), lds);
      return;
    }
    if (r < 16) { role_vu(p, c, (x - 4) * 8 + r - 8, lds); return; }
  }
  if (r >= 16) job_c(p, c, (x & 3) * 8 + ((r - 16) >> 1), (x < 4 ? 2 : 0) + (r & 1), lds);
  else if (r * 4 + c.w < 50) mixer_seed(p, c, x * 18 + r * 4 + c.w - 32);
  else if (blockIdx.x == 120 && c.w == 2) probe_pol(c);
  if (x < 4 && r < 26) { if (c.w == 3) role_mixer<true>(p, c, x * 18 + r - 8, lds); else role_mixer<false>(p, c, x * 18 + r - 8, lds); return; }
  while (rt32() - c.t0 < (x < 4 ? ADEL : FDEL)) __builtin_amdgcn_s_sleep(16);
  if (x < 4 || r < 26) { role_a(p, c, x < 4 ? (x >> 1) * 6 + r - 6 : (x >> 1) * 10 + r - 36, x & 1, x < 4 ? r - 26 : r - 16, x < 4 ? 6 : 10, lds); return; }
  for (int s = r - 26; s < 64; s += 6) if (s < 58 || !(s & 2)) job_out<9>(p, c, s, x & 3, lds);
}

extern "C" void kernel_launch(void* const* d_in, const int* in_sizes, int n_in, void* d_out, int out_size, void* d_ws, size_t ws_size,
                              hipStream_t stream) {
  Params p;
  p.ax = (const char*)d_in[0]; p.awqkv = (const char*)d_in[1]; p.awo = (const char*)d_in[2];
  p.bx = (const char*)d_in[3]; p.mem0 = (const char*)d_in[4];
  p.wq = (const char*)d_in[5]; p.wk = (const char*)d_in[6]; p.wv = (const char*)d_in[7]; p.wo = (const char*)d_in[8];
  p.out = (char*)d_out;
  p.ws = (char*)(((uintptr_t)d_ws + ((2u << 20) - 1)) & ~(uintptr_t)((2u << 20) - 1));
  (void)hipFuncSetAttribute((const void*)fused_kernel, hipFuncAttributeMaxDynamicSharedMemorySize, LDS_BYTES);
  (void)hipMemsetAsync(p.ws, 0xFF, INIT_BYTES, stream);
  hipLaunchKernelGGL(fused_kernel, dim3(256), dim3(256), LDS_BYTES, stream, p);
}
```
